```python
import torch

def _softmax(x, axis=-1):
    return torch.softmax(x, dim=axis)
def _gelu(x):
    return torch.nn.functional.gelu(x, approximate="tanh")
def _silu(x):
    return torch.nn.functional.silu(x)
def _sigmoid(x):
    return torch.sigmoid(x)
def _layernorm(x, axis=-1, eps=1e-5):
    m = x.mean(dim=axis, keepdim=True)
    v = x.var(dim=axis, keepdim=True, unbiased=False)
    return (x - m) / torch.sqrt(v + eps)
def _scatter_add(tgt, idx, src, axis=0):
    return tgt.clone().index_add_(axis, idx, src)
def _segment_sum(src, seg, n):
    out = torch.zeros((n,) + src.shape[1:], dtype=src.dtype)
    return out.index_add_(0, seg, src)
def _pad(x, pw):
    flat = tuple(v for pair in reversed(pw) for v in pair)
    return torch.nn.functional.pad(x, flat)

kernel_name = 'standalone__weight_only_int_squeezellm__prompt'

def setup_inputs():

    _w = (lambda a: (lambda t: t.to(torch.bfloat16) if t.is_floating_point() else t)(torch.as_tensor(a)))
    B, S, D, F = 1, 2048, 576, 4608
    bits = 8 if (2 <= 8 <= 4) else 4
    L = 2 ** bits
    qw = torch.randint(0, L, (D, F)).to(torch.int64)
    lut = (lambda *s: torch.randn(*s, dtype=torch.bfloat16) * 0.05)(L, F)
    return {
        "x": _w((lambda *s: torch.randn(*s, dtype=torch.bfloat16) * 0.05)(B, S, D)),
        "qw": _w(qw),
        "lut": _w(lut),
    }

def reference(x, qw, lut):
    w = (lambda x, i, axis: torch.take_along_dim(x, i, dim=axis))(lut, qw, axis=0)
    w = (lambda x, dt: x.to(dt))(w, x.dtype)
    return torch.einsum('bsd,df->bsf', x, w)
```

```cpp
#include <hip/hip_runtime.h>
#include <hip/hip_bf16.h>
typedef short bf16x8 __attribute__((ext_vector_type(8)));
typedef float f32x4 __attribute__((ext_vector_type(4)));
typedef int i32x4 __attribute__((ext_vector_type(4)));
typedef unsigned int u32x2 __attribute__((ext_vector_type(2)));
constexpr int M = 2048, K = 576, N = 4608;

__global__ __launch_bounds__(256) void k(const unsigned short* __restrict__ x, const int* __restrict__ qw,
                                         const unsigned short* __restrict__ lut, unsigned short* __restrict__ out) {
    __shared__ unsigned short slut[16 * 64];
    int bn = blockIdx.x % (N / 64), bm = blockIdx.x / (N / 64);
    int n0 = bn * 64;
    for (int i = threadIdx.x; i < 16 * 64; i += 256) slut[i] = lut[(i >> 6) * N + n0 + (i & 63)];
    __syncthreads();
    int lane = threadIdx.x & 63, w = threadIdx.x >> 6;
    int j = lane & 15, q = lane >> 4;
    int m0 = bm * 256 + w * 64;
    f32x4 acc[4][4] = {};
    for (int k0 = 0; k0 < K; k0 += 32) {
        bf16x8 a[4];
        for (int r = 0; r < 4; ++r) a[r] = *(const bf16x8*)(x + (size_t)(m0 + r * 16 + j) * K + k0 + q * 8);
        bf16x8 b[4];
        for (int i = 0; i < 8; ++i) {
            i32x4 c = *(const i32x4*)(qw + (size_t)(k0 + q * 8 + i) * N + n0 + j * 4);
            for (int t = 0; t < 4; ++t) b[t][i] = (short)slut[(c[t] & 15) * 64 + j * 4 + t];
        }
        for (int r = 0; r < 4; ++r)
            for (int t = 0; t < 4; ++t)
                acc[r][t] = __builtin_amdgcn_mfma_f32_16x16x32_bf16(a[r], b[t], acc[r][t], 0, 0, 0);
    }
    for (int r = 0; r < 4; ++r)
        for (int i = 0; i < 4; ++i) {
            int row = m0 + r * 16 + q * 4 + i;
            __hip_bfloat16 v[4];
            for (int t = 0; t < 4; ++t) v[t] = __float2bfloat16(acc[r][t][i]);
            *(u32x2*)(out + (size_t)row * N + n0 + j * 4) = *(u32x2*)v;
        }
}
extern "C" void kernel_launch(void* const* d_in, const int* in_sizes, int n_in,
                              void* d_out, int out_size, void* d_ws, size_t ws_size,
                              hipStream_t stream) {
    k<<<(N / 64) * (M / 256), 256, 0, stream>>>((const unsigned short*)d_in[0], (const int*)d_in[1],
                                                 (const unsigned short*)d_in[2], (unsigned short*)d_out);
}
```

```cpp
#include <hip/hip_runtime.h>
#include <hip/hip_bf16.h>
typedef short bf16x8 __attribute__((ext_vector_type(8)));
typedef float f32x4 __attribute__((ext_vector_type(4)));
typedef unsigned u32x4 __attribute__((ext_vector_type(4)));
typedef unsigned u32x2 __attribute__((ext_vector_type(2)));
typedef __attribute__((address_space(3))) char lds_char;

constexpr int M_ = 2048, K_ = 576, N_ = 4608;
constexpr int SLOT = 18432, NSLOT = 6;
constexpr int XR = 0, WB = NSLOT * SLOT, LT = WB + 32768, OS = LT + 8192, LDS_BYTES = OS + 12288;
constexpr int NU = 19;
#define QD 2
#define STORE_MOD " sc1"
#define SQ1 3

enum { OP_NONE = 0, OP_Q = 1, OP_X = 2, OP_S = 3 };
struct Op { int type, idx, sub; };
constexpr int MAXO = 24;
struct Prog {
    Op op[NU + 2][5][MAXO];
    int n[NU + 2][5];
    int cum[NU + 2][5];
    int cumend[NU + 2];
    int qpos[9][8];
    int xpos[NU];
};
constexpr int xfirst(int s) {
    if (s <= -2) return 0;
    if (s <= 8 - QD) return s + 2;
    int base = 8 - QD + 2, v = base + (s - (8 - QD)) * 3;
    int cap = s + 5;
    if (v > cap) v = cap;
    if (v > NU) v = NU;
    return v;
}
constexpr Prog make_prog() {
    Prog P{};
    auto add = [&](int s, int p, Op o) { P.op[s + 2][p][P.n[s + 2][p]++] = o; };
    for (int i = 0; i < 8; ++i) add(-2, 0, Op{OP_Q, 0, i});
    for (int i = 0; i < 5; ++i) add(-2, 1, Op{OP_X, 0, i});
    for (int st = 1; st < QD; ++st) for (int i = 0; i < 8; ++i) add(-2, 2 + (st - 1), Op{OP_Q, st, i});
    for (int s = -1; s < NU; ++s) {
        const int qs = s + 1 + QD;
        for (int p = 0; p < 5; ++p) {
            if (qs < 9 && p < 4) { add(s, p, Op{OP_Q, qs, 2 * p}); add(s, p, Op{OP_Q, qs, 2 * p + 1}); }
        }
        int u0 = xfirst(s), u1 = xfirst(s + 1);
        int tot = (u1 - u0) * 5, k = 0;
        for (int u = u0; u < u1; ++u) for (int i = 0; i < 5; ++i, ++k) add(s, s + 1 < 9 ? k * 5 / tot : 0, Op{OP_X, u, i});
        if (s >= 9) { for (int i = (s - 9) * SQ1; i < (s - 8) * SQ1 && i < 9; ++i) add(s, 1, Op{OP_S, i, 0}); }
        if (s >= 11) add(s, 1, Op{OP_S, s - 2, 0});
    }
    int c = 0;
    for (int s = 0; s < NU + 2; ++s) {
        for (int p = 0; p < 5; ++p) {
            P.cum[s][p] = c;
            for (int k = 0; k < P.n[s][p]; ++k) {
                ++c;
                const Op o = P.op[s][p][k];
                if (o.type == OP_Q) P.qpos[o.idx][o.sub] = c;
                if (o.type == OP_X) P.xpos[o.idx] = c;
            }
        }
        P.cumend[s] = c;
    }
    return P;
}
constexpr Prog PR = make_prog();

#define BAR() do { __builtin_amdgcn_sched_barrier(0); asm volatile("s_barrier" ::: "memory"); __builtin_amdgcn_sched_barrier(0); } while (0)
#define BAR_LGKM() do { __builtin_amdgcn_sched_barrier(0); asm volatile("s_waitcnt lgkmcnt(0)\n\ts_barrier" ::: "memory"); __builtin_amdgcn_sched_barrier(0); } while (0)

__device__ __forceinline__ unsigned cvtpk(float a, float b) {
    typedef __bf16 bf2 __attribute__((ext_vector_type(2)));
    typedef float f2 __attribute__((ext_vector_type(2)));
    f2 v = {a, b};
    bf2 r = __builtin_convertvector(v, bf2);
    return __builtin_bit_cast(unsigned, r);
}

__device__ __forceinline__ void qload(u32x4& dst, unsigned voff, __amdgpu_buffer_rsrc_t rs, unsigned soff) {
    asm volatile("buffer_load_dwordx4 %0, %1, %2, %3 offen" : "=v"(dst) : "v"(voff), "s"(rs), "s"(soff) : "memory");
}
template <int IMM> __device__ __forceinline__ void dma16(unsigned m0v, unsigned voff, __amdgpu_buffer_rsrc_t rs, unsigned soff) {
    asm volatile("s_add_u32 m0, %0, %4\n\ts_nop 0\n\tbuffer_load_dwordx4 %1, %2, %3 offen lds" :: "s"(m0v), "v"(voff), "s"(rs), "s"(soff), "i"(IMM) : "memory", "scc");
}
template <int IMM> __device__ __forceinline__ void dma16h(unsigned m0v, unsigned voff, __amdgpu_buffer_rsrc_t rs, unsigned soff) {
    asm volatile("s_add_u32 m0, %0, %4\n\ts_mov_b32 exec_hi, 0\n\tbuffer_load_dwordx4 %1, %2, %3 offen lds\n\ts_mov_b32 exec_hi, -1" :: "s"(m0v), "v"(voff), "s"(rs), "s"(soff), "i"(IMM) : "memory", "scc");
}
__device__ __forceinline__ void mfma_acc(f32x4& acc, const bf16x8& a, const bf16x8& b) {
    asm volatile("v_mfma_f32_16x16x32_bf16 %0, %1, %2, %0" : "+v"(acc) : "v"(a), "v"(b));
}
__device__ __forceinline__ void mfma_zero(f32x4& acc, const bf16x8& a, const bf16x8& b) {
    asm volatile("v_mfma_f32_16x16x32_bf16 %0, %1, %2, 0" : "=&v"(acc) : "v"(a), "v"(b));
}
template <int N> __device__ __forceinline__ void nop_on(f32x4& a, f32x4& b) { asm volatile("s_nop %2" : "+v"(a), "+v"(b) : "i"(N)); }
__device__ __forceinline__ void keep3(const bf16x8& a, const bf16x8& b, const bf16x8& c) { asm volatile("" :: "v"(a), "v"(b), "v"(c)); }
__device__ __forceinline__ int opaque_v(int v) { asm volatile("" : "+v"(v)); return v; }
__device__ __forceinline__ int opaque_s(int v) { asm volatile("" : "+s"(v)); return v; }
__device__ __forceinline__ void st16(u32x4 v, unsigned voff, __amdgpu_buffer_rsrc_t rs, unsigned soff) {
    asm volatile("buffer_store_dwordx4 %0, %1, %2, %3 offen" STORE_MOD "\n\ts_nop 1" :: "v"(v), "v"(voff), "s"(rs), "s"(soff) : "memory");
}
template <int N> __device__ __forceinline__ void waitq(u32x4 (&C)[8]) {
    asm volatile("s_waitcnt vmcnt(%8)" : "+v"(C[0]), "+v"(C[1]), "+v"(C[2]), "+v"(C[3]), "+v"(C[4]), "+v"(C[5]), "+v"(C[6]), "+v"(C[7]) : "i"(N > 63 ? 63 : N) : "memory");
}
template <int N> __device__ __forceinline__ void waitq2(u32x4& a, u32x4& b) {
    asm volatile("s_waitcnt vmcnt(%2)" : "+v"(a), "+v"(b) : "i"(N > 63 ? 63 : N) : "memory");
}
template <int N> __device__ __forceinline__ void waitv() { asm volatile("s_waitcnt vmcnt(%0)" :: "i"(N > 63 ? 63 : N) : "memory"); }
template <int N> __device__ __forceinline__ void waitl() { asm volatile("s_waitcnt lgkmcnt(%0)" :: "i"(N) : "memory"); }

#define STAMP(i)
#define STAMP_FLUSH()

#define STAMPC(i)
template <int S> struct IC { static constexpr int v = S; };
template <int I, int E, class F> __device__ __forceinline__ void sfor(F&& f) { if constexpr (I < E) { f(IC<I>{}); sfor<I + 1, E>(f); } }

__global__ __launch_bounds__(512, 2) void sq_kernel(const char* __restrict__ x, const char* __restrict__ qw,
                                                    const char* __restrict__ lut, char* __restrict__ out, unsigned long long* ws) {
    extern __shared__ __attribute__((aligned(16))) char smem[];
    lds_char* L = (lds_char*)smem;
    const int b = blockIdx.x, xcd = b & 7, idx = b >> 3;
    const int item = (xcd < 4 ? 32 * xcd : 128 + 31 * (xcd - 4)) + idx;
    const int strip = item / 7, part = item - strip * 7;
    const int nrt = 18 + (part < 2), rt0 = part * 18 + (part < 2 ? part : 2);
    const int koff = (part + 2 * strip) % 9;
    const int roff = (strip * 4) % nrt;
    const int tid = threadIdx.x, lane = tid & 63;
    const int wave = __builtin_amdgcn_readfirstlane(tid >> 6);
    auto gtile = [&](int p) { int t = p + roff; t = t >= nrt ? t - nrt : t; t = t >= nrt ? t - nrt : t; return rt0 + t; };
    auto kstage = [&](int p) { int t = p + koff; return t >= 9 ? t - 9 : t; };
    auto gtile_s = [&](int p) { int t = p + opaque_s(roff); t = t >= nrt ? t - nrt : t; t = t >= nrt ? t - nrt : t; return rt0 + t; };
    auto kstage_s = [&](int p) { int t = p + opaque_s(koff); return t >= 9 ? t - 9 : t; };
    STAMP(0);

    if (wave < 4) {
        const int c = wave;
        {
            const int code = tid >> 4, g8 = tid & 15;
            u32x4 v = *(const u32x4*)(lut + (size_t)(code * N_ + strip * 128 + g8 * 8) * 2);
            lds_char* p = L + LT + code * 512 + g8 * 8;
            *(__attribute__((address_space(3))) u32x2*)(p + 0)   = u32x2{v[0] & 0xffffu, v[2] & 0xffffu};
            *(__attribute__((address_space(3))) u32x2*)(p + 128) = u32x2{v[0] >> 16, v[2] >> 16};
            *(__attribute__((address_space(3))) u32x2*)(p + 256) = u32x2{v[1] & 0xffffu, v[3] & 0xffffu};
            *(__attribute__((address_space(3))) u32x2*)(p + 384) = u32x2{v[1] >> 16, v[3] >> 16};
        }
        BAR_LGKM();
        STAMPC(1);
        const int j = lane & 15, q = lane >> 4;
        const int fo0 = j * 128 + (((0 + q) ^ ((j >> 1) & 7)) << 4);
        const int fo1 = j * 128 + (((4 + q) ^ ((j >> 1) & 7)) << 4);
        const int fh0 = opaque_v(fo0 + 3 * SLOT), fh1 = opaque_v(fo1 + 3 * SLOT);
        const int wb0 = opaque_v(WB + c * 4096 + fo0), wb1 = opaque_v(WB + c * 4096 + fo1);
        const int wb0x = opaque_v(WB + c * 4096 + (fo0 ^ 16)), wb1x = opaque_v(WB + c * 4096 + (fo1 ^ 16));
        const int oo = j * 64 + ((q ^ ((j >> 1) & 3)) << 4);
        auto rd = [&](int off) { return *(const __attribute__((address_space(3))) bf16x8*)(L + off); };
        const int ow = opaque_v(WB + c * 4096 + oo), oz = opaque_v(OS + c * 1024 + oo);
        auto rdx = [&](int slot, int f) {
            const int base = slot < 3 ? ((f & 1) ? fo1 : fo0) : ((f & 1) ? fh1 : fh0);
            return rd(base + XR + (slot < 3 ? slot : slot - 3) * SLOT + (f >> 1) * 2048);
        };
        auto wr_out = [&](int addr, const f32x4& a0, const f32x4& a1) {
            u32x4 v = {cvtpk(a0[0], a0[1]), cvtpk(a0[2], a0[3]), cvtpk(a1[0], a1[1]), cvtpk(a1[2], a1[3])};
            *(__attribute__((address_space(3))) u32x4*)(L + addr) = v;
        };
        bf16x8 W[18][2];
        f32x4 acc[9][2];
        BAR();
        STAMPC(2);
        constexpr int PF = 6;
        bf16x8 xf[PF];
        sfor<0, 9>([&](auto U) {
            constexpr int u = U.v;
            W[2 * u][0] = rd(wb0 + (u & 1) * 16384);
            W[2 * u][1] = rd(wb0x + (u & 1) * 16384 + 2048);
            constexpr int sl = u % NSLOT;
            constexpr int P1 = 4;
            bf16x8 yf[P1];
            yf[0] = rdx(sl, 0);
            W[2 * u + 1][0] = rd(wb1 + (u & 1) * 16384);
            W[2 * u + 1][1] = rd(wb1x + (u & 1) * 16384 + 2048);
#pragma unroll
            for (int p = 1; p < P1; ++p) yf[p] = rdx(sl, p);
            __builtin_amdgcn_sched_barrier(0);
#pragma unroll
            for (int f = 0; f < 18; ++f) {
                if (u == 0 && !(f & 1)) {
                    mfma_zero(acc[f >> 1][0], W[2 * u + (f & 1)][0], yf[f % P1]);
                    mfma_zero(acc[f >> 1][1], W[2 * u + (f & 1)][1], yf[f % P1]);
                } else {
                    mfma_acc(acc[f >> 1][0], W[2 * u + (f & 1)][0], yf[f % P1]);
                    mfma_acc(acc[f >> 1][1], W[2 * u + (f & 1)][1], yf[f % P1]);
                }
                if (f + P1 < 18) yf[f % P1] = rdx(sl, f + P1);
                if (u == 8 && f >= 5 && (f & 1)) { const int i = (f - 5) >> 1; wr_out(ow + (i >> 2) * 16384 + (i & 3) * 1024, acc[i][0], acc[i][1]); }
                __builtin_amdgcn_sched_barrier(0);
            }
            if constexpr (u == 8) {
#pragma unroll
                for (int p = 0; p < PF; ++p) xf[p] = rdx(9 % NSLOT, p);
                __builtin_amdgcn_sched_barrier(0);
                nop_on<3>(acc[7][0], acc[7][1]);
                wr_out(ow + 16384 + 3 * 1024, acc[7][0], acc[7][1]);
                nop_on<3>(acc[8][0], acc[8][1]);
                wr_out(oz + 8192, acc[8][0], acc[8][1]);
                BAR_LGKM();
            } else {
                BAR();
            }
            STAMPC(3 + u);
        });
        f32x4 a2[2][2];
        sfor<9, NU>([&](auto U) {
            constexpr int u = U.v;
            constexpr int sl = u % NSLOT, nl = (u + 1) % NSLOT;
#pragma unroll
            for (int f = 0; f < 18; ++f) {
                if (f == 0) {
                    mfma_zero(a2[u & 1][0], W[f][0], xf[f % PF]);
                    mfma_zero(a2[u & 1][1], W[f][1], xf[f % PF]);
                } else {
                    mfma_acc(a2[u & 1][0], W[f][0], xf[f % PF]);
                    mfma_acc(a2[u & 1][1], W[f][1], xf[f % PF]);
                }
                if (f + PF < 18) xf[f % PF] = rdx(sl, f + PF);
                else if (u + 1 < NU) xf[f % PF] = rdx(nl, f + PF - 18);
                if (f == 3 && u > 9) wr_out(oz + ((u - 1) & 1) * 4096, a2[(u - 1) & 1][0], a2[(u - 1) & 1][1]);
                __builtin_amdgcn_sched_barrier(0);
            }
            if constexpr (u == NU - 1) {
                nop_on<8>(a2[u & 1][0], a2[u & 1][1]);
                wr_out(oz + 8192, a2[u & 1][0], a2[u & 1][1]);
                BAR_LGKM();
            } else {
                BAR();
            }
            STAMPC(3 + u);
        });
    } else {
        const int h = wave - 4;
        const __amdgpu_buffer_rsrc_t rsx = __builtin_amdgcn_make_buffer_rsrc((void*)x, (short)0, M_ * K_ * 2, 0x00020000);
        const __amdgpu_buffer_rsrc_t rsq = __builtin_amdgcn_make_buffer_rsrc((void*)qw, (short)0, K_ * N_ * 4, 0x00020000);
        const __amdgpu_buffer_rsrc_t rso = __builtin_amdgcn_make_buffer_rsrc((void*)out, (short)0, M_ * N_ * 2, 0x00020000);
        const int cq = lane & 31, g2 = lane >> 5, g = 2 * h + g2;
        unsigned vq[8];
#pragma unroll
        for (int i = 0; i < 8; ++i) vq[i] = (unsigned)((g * 8 + i) * (N_ * 4) + strip * 512 + cq * 16);
        u32x4 Q[QD][8];
        const unsigned m0b = (unsigned)(h * 4608);
        unsigned vx1[5], vx2[5];
#pragma unroll
        for (int i = 0; i < 5; ++i) {
            int rho = 36 * h + 8 * i + (lane >> 3);
            if (rho > 143) rho = 143;
            const int piece = rho >> 4, r = rho & 15, sc = (lane & 7) ^ ((r >> 1) & 7);
            vx1[i] = (unsigned)((gtile(piece) * 16 + r) * (K_ * 2) + sc * 16);
            vx2[i] = (unsigned)(r * (K_ * 2) + kstage(piece) * 128 + sc * 16);
        }
        const int srow = 4 * h + (lane >> 4), scc = lane & 15;
        const int so_in = srow * 64 + (((scc & 3) ^ ((srow >> 1) & 3)) << 4);
        const int sa_wb = opaque_v(WB + (scc >> 2) * 4096 + so_in), sa_os = opaque_v(OS + (scc >> 2) * 1024 + so_in);
        const unsigned vo = (unsigned)(srow * (N_ * 2) + strip * 256 + scc * 16);
        auto tile_lds = [&](int p) { return p < 8 ? sa_wb + (p >> 2) * 16384 + (p & 3) * 1024 : (p == 8 || p == NU - 1) ? sa_os + 8192 : sa_os + (p & 1) * 4096; };
        auto rd_tile = [&](int p) { return *(const __attribute__((address_space(3))) u32x4*)(L + tile_lds(p)); };
        u32x4 SV[MAXO];
        unsigned soq[9], sox[NU];
        auto run_ops = [&](auto S, auto Pp) {
            constexpr int s = S.v, p = Pp.v;
            sfor<0, PR.n[s + 2][p]>([&](auto Kk) {
                constexpr Op o = PR.op[s + 2][p][Kk.v];
                if constexpr (o.type == OP_Q) {
                    if constexpr (o.sub == 0) soq[o.idx] = (unsigned)(kstage_s(o.idx) * 64 * (N_ * 4));
                    qload(Q[o.idx % QD][o.sub], vq[o.sub], rsq, soq[o.idx]);
                } else if constexpr (o.type == OP_X) {
                    constexpr int u = o.idx, i = o.sub;
                    if constexpr (i == 0) sox[u] = u < 9 ? (unsigned)(kstage_s(u) * 128) : (unsigned)(gtile_s(u) * SLOT);
                    constexpr int sb = XR + (u % NSLOT) * SLOT + i * 1024;
                    if constexpr (i < 4) dma16<sb>(m0b, u < 9 ? vx1[i] : vx2[i], rsx, sox[u]);
                    else dma16h<sb>(m0b, u < 9 ? vx1[i] : vx2[i], rsx, sox[u]);
                } else if constexpr (o.type == OP_S) {
                    st16(SV[Kk.v], vo, rso, (unsigned)(gtile_s(o.idx) * 16 * N_ * 2));
                }
            });
            __builtin_amdgcn_sched_barrier(0);
        };
        sfor<0, 5>([&](auto Pp) { run_ops(IC<-2>{}, Pp); });
        STAMP(1);
        BAR();
        const int m = (cq >> 1) & 3, t = cq & 1;
        const int wa = WB + (cq >> 3) * 4096 + t * 2048 + m * 512;
        const int wa0 = opaque_v(wa + ((g ^ t ^ (2 * m)) << 4)), wa1 = opaque_v(wa + ((g ^ t ^ (2 * m + 1)) << 4));
        const int ltb = opaque_v(LT + cq * 4);
        sfor<-1, NU>([&](auto S) {
            constexpr int s = S.v;
            if constexpr (s + 1 < 9) {
                constexpr int st = s + 1;
                u32x4 (&C)[8] = Q[st % QD];
                unsigned lk[4][8];
                u32x4 w[4];
                sfor<0, 5>([&](auto Pp) {
                    constexpr int p = Pp.v;
                    if constexpr (p < 4) {
                        waitq2<PR.cum[s + 2][p] - PR.qpos[st][2 * p + 1]>(C[2 * p], C[2 * p + 1]);
#pragma unroll
                        for (int e = 0; e < 4; ++e) {
                            lk[p][2 * e] = *(const __attribute__((address_space(3))) unsigned*)(L + ltb + (C[2 * p][e] << 9) + e * 128);
                            lk[p][2 * e + 1] = *(const __attribute__((address_space(3))) unsigned*)(L + ltb + (C[2 * p + 1][e] << 9) + e * 128);
                        }
                        __builtin_amdgcn_sched_barrier(0);
                    }
                    if constexpr (p > 0) {
#pragma unroll
                        for (int e = 0; e < 4; ++e) w[e][p - 1] = lk[p - 1][2 * e] | (lk[p - 1][2 * e + 1] << 16);
                    }
                    if constexpr (p == 4) {
#pragma unroll
                        for (int e = 0; e < 4; ++e)
                            *(__attribute__((address_space(3))) u32x4*)(L + (e < 2 ? wa0 : wa1) + (st & 1) * 16384 + e * 128) = w[e];
                    }
                    __builtin_amdgcn_sched_barrier(0);
                    run_ops(S, Pp);
                });
            } else {
                sfor<0, PR.n[s + 2][1]>([&](auto Kk) { SV[Kk.v] = rd_tile(PR.op[s + 2][1][Kk.v].idx); });
                __builtin_amdgcn_sched_barrier(0);
                run_ops(S, IC<0>{});
                run_ops(S, IC<1>{});
            }
            constexpr int need0 = (s >= 7) ? s + 2 : s + 1;
            constexpr int need = need0 > NU - 1 ? NU - 1 : need0;
            waitv<PR.cumend[s + 2] - PR.xpos[need]>();
            BAR_LGKM();
            STAMP(3 + s);
        });
        {
            u32x4 v0 = rd_tile(NU - 2), v1 = rd_tile(NU - 1);
            st16(v0, vo, rso, (unsigned)(gtile_s(NU - 2) * 16 * N_ * 2));
            if (nrt == NU) st16(v1, vo, rso, (unsigned)(gtile_s(NU - 1) * 16 * N_ * 2));
        }
    }
}

extern "C" void kernel_launch(void* const* d_in, const int* in_sizes, int n_in,
                              void* d_out, int out_size, void* d_ws, size_t ws_size,
                              hipStream_t stream) {
    (void)hipFuncSetAttribute((const void*)sq_kernel, hipFuncAttributeMaxDynamicSharedMemorySize, LDS_BYTES);
    sq_kernel<<<252, 512, LDS_BYTES, stream>>>((const char*)d_in[0], (const char*)d_in[1], (const char*)d_in[2], (char*)d_out, (unsigned long long*)d_ws);
}
```

```cpp
#include <hip/hip_runtime.h>
#include <hip/hip_bf16.h>
typedef short bf16x8 __attribute__((ext_vector_type(8)));
typedef float f32x4 __attribute__((ext_vector_type(4)));
typedef unsigned u32x4 __attribute__((ext_vector_type(4)));
typedef unsigned u32x2 __attribute__((ext_vector_type(2)));
typedef __attribute__((address_space(3))) char lds_char;

constexpr int M_ = 2048, K_ = 576, N_ = 4608;
constexpr int SLOT = 18432, NSLOT = 6;
constexpr int XR = 0, WB = NSLOT * SLOT, LT = WB + 32768, OS = LT + 8192, LDS_BYTES = OS + 12288;
constexpr int NU = 19;
#define QD 2
#define STORE_MOD " sc1"
#define SQ1 3

enum { OP_NONE = 0, OP_Q = 1, OP_X = 2, OP_S = 3 };
struct Op { int type, idx, sub; };
constexpr int MAXO = 24;
struct Prog {
    Op op[NU + 2][5][MAXO];
    int n[NU + 2][5];
    int cum[NU + 2][5];
    int cumend[NU + 2];
    int qpos[9][8];
    int xpos[NU];
};
constexpr int xfirst(int s) {
    if (s <= -2) return 0;
    if (s <= 8 - QD) return s + 2;
    int base = 8 - QD + 2, v = base + (s - (8 - QD)) * 3;
    int cap = s + 5;
    if (v > cap) v = cap;
    if (v > NU) v = NU;
    return v;
}
constexpr Prog make_prog() {
    Prog P{};
    auto add = [&](int s, int p, Op o) { P.op[s + 2][p][P.n[s + 2][p]++] = o; };
    for (int i = 0; i < 8; ++i) add(-2, 0, Op{OP_Q, 0, i});
    for (int i = 0; i < 5; ++i) add(-2, 1, Op{OP_X, 0, i});
    for (int st = 1; st < QD; ++st) for (int i = 0; i < 8; ++i) add(-2, 2 + (st - 1), Op{OP_Q, st, i});
    for (int s = -1; s < NU; ++s) {
        const int qs = s + 1 + QD;
        for (int p = 0; p < 5; ++p) {
            if (qs < 9 && p < 4) { add(s, p, Op{OP_Q, qs, 2 * p}); add(s, p, Op{OP_Q, qs, 2 * p + 1}); }
        }
        int u0 = xfirst(s), u1 = xfirst(s + 1);
        int tot = (u1 - u0) * 5, k = 0;
        for (int u = u0; u < u1; ++u) for (int i = 0; i < 5; ++i, ++k) add(s, s + 1 < 9 ? k * 5 / tot : 0, Op{OP_X, u, i});
        if (s >= 9) { for (int i = (s - 9) * SQ1; i < (s - 8) * SQ1 && i < 9; ++i) add(s, 1, Op{OP_S, i, 0}); }
        if (s >= 11) add(s, 1, Op{OP_S, s - 2, 0});
    }
    int c = 0;
    for (int s = 0; s < NU + 2; ++s) {
        for (int p = 0; p < 5; ++p) {
            P.cum[s][p] = c;
            for (int k = 0; k < P.n[s][p]; ++k) {
                ++c;
                const Op o = P.op[s][p][k];
                if (o.type == OP_Q) P.qpos[o.idx][o.sub] = c;
                if (o.type == OP_X) P.xpos[o.idx] = c;
            }
        }
        P.cumend[s] = c;
    }
    return P;
}
constexpr Prog PR = make_prog();

#define BAR() do { __builtin_amdgcn_sched_barrier(0); asm volatile("s_barrier" ::: "memory"); __builtin_amdgcn_sched_barrier(0); } while (0)
#define BAR_LGKM() do { __builtin_amdgcn_sched_barrier(0); asm volatile("s_waitcnt lgkmcnt(0)\n\ts_barrier" ::: "memory"); __builtin_amdgcn_sched_barrier(0); } while (0)

__device__ __forceinline__ unsigned cvtpk(float a, float b) {
    typedef __bf16 bf2 __attribute__((ext_vector_type(2)));
    typedef float f2 __attribute__((ext_vector_type(2)));
    f2 v = {a, b};
    bf2 r = __builtin_convertvector(v, bf2);
    return __builtin_bit_cast(unsigned, r);
}

__device__ __forceinline__ void qload(u32x4& dst, unsigned voff, __amdgpu_buffer_rsrc_t rs, unsigned soff) {
    asm volatile("buffer_load_dwordx4 %0, %1, %2, %3 offen" : "=v"(dst) : "v"(voff), "s"(rs), "s"(soff) : "memory");
}
template <int IMM> __device__ __forceinline__ void dma16(unsigned m0v, unsigned voff, __amdgpu_buffer_rsrc_t rs, unsigned soff) {
    asm volatile("s_add_u32 m0, %0, %4\n\ts_nop 0\n\tbuffer_load_dwordx4 %1, %2, %3 offen lds" :: "s"(m0v), "v"(voff), "s"(rs), "s"(soff), "i"(IMM) : "memory", "scc");
}
template <int IMM> __device__ __forceinline__ void dma16h(unsigned m0v, unsigned voff, __amdgpu_buffer_rsrc_t rs, unsigned soff) {
    asm volatile("s_add_u32 m0, %0, %4\n\ts_mov_b32 exec_hi, 0\n\tbuffer_load_dwordx4 %1, %2, %3 offen lds\n\ts_mov_b32 exec_hi, -1" :: "s"(m0v), "v"(voff), "s"(rs), "s"(soff), "i"(IMM) : "memory", "scc");
}
__device__ __forceinline__ void mfma_acc(f32x4& acc, const bf16x8& a, const bf16x8& b) {
    asm volatile("v_mfma_f32_16x16x32_bf16 %0, %1, %2, %0" : "+v"(acc) : "v"(a), "v"(b));
}
__device__ __forceinline__ void mfma_zero(f32x4& acc, const bf16x8& a, const bf16x8& b) {
    asm volatile("v_mfma_f32_16x16x32_bf16 %0, %1, %2, 0" : "=&v"(acc) : "v"(a), "v"(b));
}
template <int N> __device__ __forceinline__ void nop_on(f32x4& a, f32x4& b) { asm volatile("s_nop %2" : "+v"(a), "+v"(b) : "i"(N)); }
__device__ __forceinline__ void keep3(const bf16x8& a, const bf16x8& b, const bf16x8& c) { asm volatile("" :: "v"(a), "v"(b), "v"(c)); }
__device__ __forceinline__ int opaque_v(int v) { asm volatile("" : "+v"(v)); return v; }
__device__ __forceinline__ int opaque_s(int v) { asm volatile("" : "+s"(v)); return v; }
__device__ __forceinline__ void st16(u32x4 v, unsigned voff, __amdgpu_buffer_rsrc_t rs, unsigned soff) {
    asm volatile("buffer_store_dwordx4 %0, %1, %2, %3 offen" STORE_MOD "\n\ts_nop 1" :: "v"(v), "v"(voff), "s"(rs), "s"(soff) : "memory");
}
template <int N> __device__ __forceinline__ void waitq(u32x4 (&C)[8]) {
    asm volatile("s_waitcnt vmcnt(%8)" : "+v"(C[0]), "+v"(C[1]), "+v"(C[2]), "+v"(C[3]), "+v"(C[4]), "+v"(C[5]), "+v"(C[6]), "+v"(C[7]) : "i"(N > 63 ? 63 : N) : "memory");
}
template <int N> __device__ __forceinline__ void waitq2(u32x4& a, u32x4& b) {
    asm volatile("s_waitcnt vmcnt(%2)" : "+v"(a), "+v"(b) : "i"(N > 63 ? 63 : N) : "memory");
}
template <int N> __device__ __forceinline__ void waitv() { asm volatile("s_waitcnt vmcnt(%0)" :: "i"(N > 63 ? 63 : N) : "memory"); }
template <int N> __device__ __forceinline__ void waitl() { asm volatile("s_waitcnt lgkmcnt(%0)" :: "i"(N) : "memory"); }

#define AMP 0
__device__ __forceinline__ unsigned long long stamp_now() { unsigned long long t_; asm volatile("s_memtime %0\n\ts_waitcnt lgkmcnt(0)" : "=s"(t_) :: "memory"); return t_; }
#define STAMPS
#define STAMP(i) do { if ((i) == AMP) stampv = (unsigned)(stamp_now() - tbase); } while (0)
#define STAMP_FLUSH() do { unsigned long long te_ = stamp_now(); while (stamp_now() - te_ < 4ull * stampv) __builtin_amdgcn_s_sleep(1); } while (0)

#define STAMPC(i)
template <int S> struct IC { static constexpr int v = S; };
template <int I, int E, class F> __device__ __forceinline__ void sfor(F&& f) { if constexpr (I < E) { f(IC<I>{}); sfor<I + 1, E>(f); } }

__global__ __launch_bounds__(512, 2) void sq_kernel(const char* __restrict__ x, const char* __restrict__ qw,
                                                    const char* __restrict__ lut, char* __restrict__ out, unsigned long long* ws) {
    extern __shared__ __attribute__((aligned(16))) char smem[];
    lds_char* L = (lds_char*)smem;
    const int b = blockIdx.x, xcd = b & 7, idx = b >> 3;
    const int item = (xcd < 4 ? 32 * xcd : 128 + 31 * (xcd - 4)) + idx;
    const int strip = item / 7, part = item - strip * 7;
    const int nrt = 18 + (part < 2), rt0 = part * 18 + (part < 2 ? part : 2);
    const int koff = (part + 2 * strip) % 9;
    const int roff = (strip * 4) % nrt;
    const int tid = threadIdx.x, lane = tid & 63;
    const int wave = __builtin_amdgcn_readfirstlane(tid >> 6);
    auto gtile = [&](int p) { int t = p + roff; t = t >= nrt ? t - nrt : t; t = t >= nrt ? t - nrt : t; return rt0 + t; };
    auto kstage = [&](int p) { int t = p + koff; return t >= 9 ? t - 9 : t; };
    auto gtile_s = [&](int p) { int t = p + opaque_s(roff); t = t >= nrt ? t - nrt : t; t = t >= nrt ? t - nrt : t; return rt0 + t; };
    auto kstage_s = [&](int p) { int t = p + opaque_s(koff); return t >= 9 ? t - 9 : t; };
    const unsigned long long tbase = __builtin_amdgcn_s_memtime();
    unsigned stampv = 0;
    STAMP(0);

    if (wave < 4) {
        const int c = wave;
        {
            const int code = tid >> 4, g8 = tid & 15;
            u32x4 v = *(const u32x4*)(lut + (size_t)(code * N_ + strip * 128 + g8 * 8) * 2);
            lds_char* p = L + LT + code * 512 + g8 * 8;
            *(__attribute__((address_space(3))) u32x2*)(p + 0)   = u32x2{v[0] & 0xffffu, v[2] & 0xffffu};
            *(__attribute__((address_space(3))) u32x2*)(p + 128) = u32x2{v[0] >> 16, v[2] >> 16};
            *(__attribute__((address_space(3))) u32x2*)(p + 256) = u32x2{v[1] & 0xffffu, v[3] & 0xffffu};
            *(__attribute__((address_space(3))) u32x2*)(p + 384) = u32x2{v[1] >> 16, v[3] >> 16};
        }
        BAR_LGKM();
        STAMPC(1);
        const int j = lane & 15, q = lane >> 4;
        const int fo0 = j * 128 + (((0 + q) ^ ((j >> 1) & 7)) << 4);
        const int fo1 = j * 128 + (((4 + q) ^ ((j >> 1) & 7)) << 4);
        const int fh0 = opaque_v(fo0 + 3 * SLOT), fh1 = opaque_v(fo1 + 3 * SLOT);
        const int wb0 = opaque_v(WB + c * 4096 + fo0), wb1 = opaque_v(WB + c * 4096 + fo1);
        const int wb0x = opaque_v(WB + c * 4096 + (fo0 ^ 16)), wb1x = opaque_v(WB + c * 4096 + (fo1 ^ 16));
        const int oo = j * 64 + ((q ^ ((j >> 1) & 3)) << 4);
        auto rd = [&](int off) { return *(const __attribute__((address_space(3))) bf16x8*)(L + off); };
        const int ow = opaque_v(WB + c * 4096 + oo), oz = opaque_v(OS + c * 1024 + oo);
        auto rdx = [&](int slot, int f) {
            const int base = slot < 3 ? ((f & 1) ? fo1 : fo0) : ((f & 1) ? fh1 : fh0);
            return rd(base + XR + (slot < 3 ? slot : slot - 3) * SLOT + (f >> 1) * 2048);
        };
        auto wr_out = [&](int addr, const f32x4& a0, const f32x4& a1) {
            u32x4 v = {cvtpk(a0[0], a0[1]), cvtpk(a0[2], a0[3]), cvtpk(a1[0], a1[1]), cvtpk(a1[2], a1[3])};
            *(__attribute__((address_space(3))) u32x4*)(L + addr) = v;
        };
        bf16x8 W[18][2];
        f32x4 acc[9][2];
        BAR();
        STAMPC(2);
        constexpr int PF = 6;
        bf16x8 xf[PF];
        sfor<0, 9>([&](auto U) {
            constexpr int u = U.v;
            W[2 * u][0] = rd(wb0 + (u & 1) * 16384);
            W[2 * u][1] = rd(wb0x + (u & 1) * 16384 + 2048);
            constexpr int sl = u % NSLOT;
            constexpr int P1 = 4;
            bf16x8 yf[P1];
            yf[0] = rdx(sl, 0);
            W[2 * u + 1][0] = rd(wb1 + (u & 1) * 16384);
            W[2 * u + 1][1] = rd(wb1x + (u & 1) * 16384 + 2048);
#pragma unroll
            for (int p = 1; p < P1; ++p) yf[p] = rdx(sl, p);
            __builtin_amdgcn_sched_barrier(0);
#pragma unroll
            for (int f = 0; f < 18; ++f) {
                if (u == 0 && !(f & 1)) {
                    mfma_zero(acc[f >> 1][0], W[2 * u + (f & 1)][0], yf[f % P1]);
                    mfma_zero(acc[f >> 1][1], W[2 * u + (f & 1)][1], yf[f % P1]);
                } else {
                    mfma_acc(acc[f >> 1][0], W[2 * u + (f & 1)][0], yf[f % P1]);
                    mfma_acc(acc[f >> 1][1], W[2 * u + (f & 1)][1], yf[f % P1]);
                }
                if (f + P1 < 18) yf[f % P1] = rdx(sl, f + P1);
                if (u == 8 && f >= 5 && (f & 1)) { const int i = (f - 5) >> 1; wr_out(ow + (i >> 2) * 16384 + (i & 3) * 1024, acc[i][0], acc[i][1]); }
                __builtin_amdgcn_sched_barrier(0);
            }
            if constexpr (u == 8) {
#pragma unroll
                for (int p = 0; p < PF; ++p) xf[p] = rdx(9 % NSLOT, p);
                __builtin_amdgcn_sched_barrier(0);
                nop_on<3>(acc[7][0], acc[7][1]);
                wr_out(ow + 16384 + 3 * 1024, acc[7][0], acc[7][1]);
                nop_on<3>(acc[8][0], acc[8][1]);
                wr_out(oz + 8192, acc[8][0], acc[8][1]);
                BAR_LGKM();
            } else {
                BAR();
            }
            STAMPC(3 + u);
        });
        f32x4 a2[2][2];
        sfor<9, NU>([&](auto U) {
            constexpr int u = U.v;
            constexpr int sl = u % NSLOT, nl = (u + 1) % NSLOT;
#pragma unroll
            for (int f = 0; f < 18; ++f) {
                if (f == 0) {
                    mfma_zero(a2[u & 1][0], W[f][0], xf[f % PF]);
                    mfma_zero(a2[u & 1][1], W[f][1], xf[f % PF]);
                } else {
                    mfma_acc(a2[u & 1][0], W[f][0], xf[f % PF]);
                    mfma_acc(a2[u & 1][1], W[f][1], xf[f % PF]);
                }
                if (f + PF < 18) xf[f % PF] = rdx(sl, f + PF);
                else if (u + 1 < NU) xf[f % PF] = rdx(nl, f + PF - 18);
                if (f == 3 && u > 9) wr_out(oz + ((u - 1) & 1) * 4096, a2[(u - 1) & 1][0], a2[(u - 1) & 1][1]);
                __builtin_amdgcn_sched_barrier(0);
            }
            if constexpr (u == NU - 1) {
                nop_on<8>(a2[u & 1][0], a2[u & 1][1]);
                wr_out(oz + 8192, a2[u & 1][0], a2[u & 1][1]);
                BAR_LGKM();
            } else {
                BAR();
            }
            STAMPC(3 + u);
        });
    } else {
        const int h = wave - 4;
        const __amdgpu_buffer_rsrc_t rsx = __builtin_amdgcn_make_buffer_rsrc((void*)x, (short)0, M_ * K_ * 2, 0x00020000);
        const __amdgpu_buffer_rsrc_t rsq = __builtin_amdgcn_make_buffer_rsrc((void*)qw, (short)0, K_ * N_ * 4, 0x00020000);
        const __amdgpu_buffer_rsrc_t rso = __builtin_amdgcn_make_buffer_rsrc((void*)out, (short)0, M_ * N_ * 2, 0x00020000);
        const int cq = lane & 31, g2 = lane >> 5, g = 2 * h + g2;
        unsigned vq[8];
#pragma unroll
        for (int i = 0; i < 8; ++i) vq[i] = (unsigned)((g * 8 + i) * (N_ * 4) + strip * 512 + cq * 16);
        u32x4 Q[QD][8];
        const unsigned m0b = (unsigned)(h * 4608);
        unsigned vx1[5], vx2[5];
#pragma unroll
        for (int i = 0; i < 5; ++i) {
            int rho = 36 * h + 8 * i + (lane >> 3);
            if (rho > 143) rho = 143;
            const int piece = rho >> 4, r = rho & 15, sc = (lane & 7) ^ ((r >> 1) & 7);
            vx1[i] = (unsigned)((gtile(piece) * 16 + r) * (K_ * 2) + sc * 16);
            vx2[i] = (unsigned)(r * (K_ * 2) + kstage(piece) * 128 + sc * 16);
        }
        const int srow = 4 * h + (lane >> 4), scc = lane & 15;
        const int so_in = srow * 64 + (((scc & 3) ^ ((srow >> 1) & 3)) << 4);
        const int sa_wb = opaque_v(WB + (scc >> 2) * 4096 + so_in), sa_os = opaque_v(OS + (scc >> 2) * 1024 + so_in);
        const unsigned vo = (unsigned)(srow * (N_ * 2) + strip * 256 + scc * 16);
        auto tile_lds = [&](int p) { return p < 8 ? sa_wb + (p >> 2) * 16384 + (p & 3) * 1024 : (p == 8 || p == NU - 1) ? sa_os + 8192 : sa_os + (p & 1) * 4096; };
        auto rd_tile = [&](int p) { return *(const __attribute__((address_space(3))) u32x4*)(L + tile_lds(p)); };
        u32x4 SV[MAXO];
        unsigned soq[9], sox[NU];
        auto run_ops = [&](auto S, auto Pp) {
            constexpr int s = S.v, p = Pp.v;
            sfor<0, PR.n[s + 2][p]>([&](auto Kk) {
                constexpr Op o = PR.op[s + 2][p][Kk.v];
                if constexpr (o.type == OP_Q) {
                    if constexpr (o.sub == 0) soq[o.idx] = (unsigned)(kstage_s(o.idx) * 64 * (N_ * 4));
                    qload(Q[o.idx % QD][o.sub], vq[o.sub], rsq, soq[o.idx]);
                } else if constexpr (o.type == OP_X) {
                    constexpr int u = o.idx, i = o.sub;
                    if constexpr (i == 0) sox[u] = u < 9 ? (unsigned)(kstage_s(u) * 128) : (unsigned)(gtile_s(u) * SLOT);
                    constexpr int sb = XR + (u % NSLOT) * SLOT + i * 1024;
                    if constexpr (i < 4) dma16<sb>(m0b, u < 9 ? vx1[i] : vx2[i], rsx, sox[u]);
                    else dma16h<sb>(m0b, u < 9 ? vx1[i] : vx2[i], rsx, sox[u]);
                } else if constexpr (o.type == OP_S) {
                    st16(SV[Kk.v], vo, rso, (unsigned)(gtile_s(o.idx) * 16 * N_ * 2));
                }
            });
            __builtin_amdgcn_sched_barrier(0);
        };
        sfor<0, 5>([&](auto Pp) { run_ops(IC<-2>{}, Pp); });
        STAMP(1);
        BAR();
        const int m = (cq >> 1) & 3, t = cq & 1;
        const int wa = WB + (cq >> 3) * 4096 + t * 2048 + m * 512;
        const int wa0 = opaque_v(wa + ((g ^ t ^ (2 * m)) << 4)), wa1 = opaque_v(wa + ((g ^ t ^ (2 * m + 1)) << 4));
        const int ltb = opaque_v(LT + cq * 4);
        sfor<-1, NU>([&](auto S) {
            constexpr int s = S.v;
            if constexpr (s + 1 < 9) {
                constexpr int st = s + 1;
                u32x4 (&C)[8] = Q[st % QD];
                unsigned lk[4][8];
                u32x4 w[4];
                sfor<0, 5>([&](auto Pp) {
                    constexpr int p = Pp.v;
                    if constexpr (p < 4) {
                        waitq2<PR.cum[s + 2][p] - PR.qpos[st][2 * p + 1]>(C[2 * p], C[2 * p + 1]);
#pragma unroll
                        for (int e = 0; e < 4; ++e) {
                            lk[p][2 * e] = *(const __attribute__((address_space(3))) unsigned*)(L + ltb + (C[2 * p][e] << 9) + e * 128);
                            lk[p][2 * e + 1] = *(const __attribute__((address_space(3))) unsigned*)(L + ltb + (C[2 * p + 1][e] << 9) + e * 128);
                        }
                        __builtin_amdgcn_sched_barrier(0);
                    }
                    if constexpr (p > 0) {
#pragma unroll
                        for (int e = 0; e < 4; ++e) w[e][p - 1] = lk[p - 1][2 * e] | (lk[p - 1][2 * e + 1] << 16);
                    }
                    if constexpr (p == 4) {
#pragma unroll
                        for (int e = 0; e < 4; ++e)
                            *(__attribute__((address_space(3))) u32x4*)(L + (e < 2 ? wa0 : wa1) + (st & 1) * 16384 + e * 128) = w[e];
                    }
                    __builtin_amdgcn_sched_barrier(0);
                    run_ops(S, Pp);
                });
            } else {
                sfor<0, PR.n[s + 2][1]>([&](auto Kk) { SV[Kk.v] = rd_tile(PR.op[s + 2][1][Kk.v].idx); });
                __builtin_amdgcn_sched_barrier(0);
                run_ops(S, IC<0>{});
                run_ops(S, IC<1>{});
            }
            constexpr int need0 = (s >= 7) ? s + 2 : s + 1;
            constexpr int need = need0 > NU - 1 ? NU - 1 : need0;
            waitv<PR.cumend[s + 2] - PR.xpos[need]>();
            BAR_LGKM();
            STAMP(3 + s);
        });
        {
            u32x4 v0 = rd_tile(NU - 2), v1 = rd_tile(NU - 1);
            st16(v0, vo, rso, (unsigned)(gtile_s(NU - 2) * 16 * N_ * 2));
            if (nrt == NU) st16(v1, vo, rso, (unsigned)(gtile_s(NU - 1) * 16 * N_ * 2));
        }
        asm volatile("s_waitcnt vmcnt(0)" ::: "memory");
        STAMP(3 + NU);
        STAMP_FLUSH();
    }
}

extern "C" void kernel_launch(void* const* d_in, const int* in_sizes, int n_in,
                              void* d_out, int out_size, void* d_ws, size_t ws_size,
                              hipStream_t stream) {
    (void)hipFuncSetAttribute((const void*)sq_kernel, hipFuncAttributeMaxDynamicSharedMemorySize, LDS_BYTES);
    sq_kernel<<<252, 512, LDS_BYTES, stream>>>((const char*)d_in[0], (const char*)d_in[1], (const char*)d_in[2], (char*)d_out, (unsigned long long*)d_ws);
}
```

```cpp
#define STORE_MOD " nt"
#include <hip/hip_runtime.h>
#include <hip/hip_bf16.h>
typedef short bf16x8 __attribute__((ext_vector_type(8)));
typedef float f32x4 __attribute__((ext_vector_type(4)));
typedef unsigned u32x4 __attribute__((ext_vector_type(4)));
typedef unsigned u32x2 __attribute__((ext_vector_type(2)));
typedef __attribute__((address_space(3))) char lds_char;

constexpr int M_ = 2048, K_ = 576, N_ = 4608;
constexpr int SLOT = 18432, NSLOT = 6;
constexpr int XR = 0, WB = NSLOT * SLOT, LT = WB + 32768, OS = LT + 8192, LDS_BYTES = OS + 12288;
constexpr int NU = 19;
#define QD 2
#define SQ1 3

enum { OP_NONE = 0, OP_Q = 1, OP_X = 2, OP_S = 3 };
struct Op { int type, idx, sub; };
constexpr int MAXO = 24;
struct Prog {
    Op op[NU + 2][5][MAXO];
    int n[NU + 2][5];
    int cum[NU + 2][5];
    int cumend[NU + 2];
    int qpos[9][8];
    int xpos[NU];
};
constexpr int xfirst(int s) {
    if (s <= -2) return 0;
    if (s <= 8 - QD) return s + 2;
    int base = 8 - QD + 2, v = base + (s - (8 - QD)) * 3;
    int cap = s + 5;
    if (v > cap) v = cap;
    if (v > NU) v = NU;
    return v;
}
constexpr Prog make_prog() {
    Prog P{};
    auto add = [&](int s, int p, Op o) { P.op[s + 2][p][P.n[s + 2][p]++] = o; };
    for (int i = 0; i < 8; ++i) add(-2, 0, Op{OP_Q, 0, i});
    for (int i = 0; i < 5; ++i) add(-2, 1, Op{OP_X, 0, i});
    for (int st = 1; st < QD; ++st) for (int i = 0; i < 8; ++i) add(-2, 2 + (st - 1), Op{OP_Q, st, i});
    for (int s = -1; s < NU; ++s) {
        const int qs = s + 1 + QD;
        for (int p = 0; p < 5; ++p) {
            if (qs < 9 && p < 4) { add(s, p, Op{OP_Q, qs, 2 * p}); add(s, p, Op{OP_Q, qs, 2 * p + 1}); }
        }
        int u0 = xfirst(s), u1 = xfirst(s + 1);
        int tot = (u1 - u0) * 5, k = 0;
        for (int u = u0; u < u1; ++u) for (int i = 0; i < 5; ++i, ++k) add(s, s + 1 < 9 ? k * 5 / tot : 0, Op{OP_X, u, i});
        if (s >= 9) { for (int i = (s - 9) * SQ1; i < (s - 8) * SQ1 && i < 9; ++i) add(s, 1, Op{OP_S, i, 0}); }
        if (s >= 11) add(s, 1, Op{OP_S, s - 2, 0});
    }
    int c = 0;
    for (int s = 0; s < NU + 2; ++s) {
        for (int p = 0; p < 5; ++p) {
            P.cum[s][p] = c;
            for (int k = 0; k < P.n[s][p]; ++k) {
                ++c;
                const Op o = P.op[s][p][k];
                if (o.type == OP_Q) P.qpos[o.idx][o.sub] = c;
                if (o.type == OP_X) P.xpos[o.idx] = c;
            }
        }
        P.cumend[s] = c;
    }
    return P;
}
constexpr Prog PR = make_prog();

#define BAR() do { __builtin_amdgcn_sched_barrier(0); asm volatile("s_barrier" ::: "memory"); __builtin_amdgcn_sched_barrier(0); } while (0)
#define BAR_LGKM() do { __builtin_amdgcn_sched_barrier(0); asm volatile("s_waitcnt lgkmcnt(0)\n\ts_barrier" ::: "memory"); __builtin_amdgcn_sched_barrier(0); } while (0)

__device__ __forceinline__ unsigned cvtpk(float a, float b) {
    typedef __bf16 bf2 __attribute__((ext_vector_type(2)));
    typedef float f2 __attribute__((ext_vector_type(2)));
    f2 v = {a, b};
    bf2 r = __builtin_convertvector(v, bf2);
    return __builtin_bit_cast(unsigned, r);
}

__device__ __forceinline__ void qload(u32x4& dst, unsigned voff, __amdgpu_buffer_rsrc_t rs, unsigned soff) {
    asm volatile("buffer_load_dwordx4 %0, %1, %2, %3 offen" : "=v"(dst) : "v"(voff), "s"(rs), "s"(soff) : "memory");
}
template <int IMM> __device__ __forceinline__ void dma16(unsigned m0v, unsigned voff, __amdgpu_buffer_rsrc_t rs, unsigned soff) {
    asm volatile("s_add_u32 m0, %0, %4\n\ts_nop 0\n\tbuffer_load_dwordx4 %1, %2, %3 offen lds" :: "s"(m0v), "v"(voff), "s"(rs), "s"(soff), "i"(IMM) : "memory", "scc");
}
template <int IMM> __device__ __forceinline__ void dma16h(unsigned m0v, unsigned voff, __amdgpu_buffer_rsrc_t rs, unsigned soff) {
    asm volatile("s_add_u32 m0, %0, %4\n\ts_mov_b32 exec_hi, 0\n\tbuffer_load_dwordx4 %1, %2, %3 offen lds\n\ts_mov_b32 exec_hi, -1" :: "s"(m0v), "v"(voff), "s"(rs), "s"(soff), "i"(IMM) : "memory", "scc");
}
__device__ __forceinline__ void mfma_acc(f32x4& acc, const bf16x8& a, const bf16x8& b) {
    asm volatile("v_mfma_f32_16x16x32_bf16 %0, %1, %2, %0" : "+v"(acc) : "v"(a), "v"(b));
}
__device__ __forceinline__ void mfma_zero(f32x4& acc, const bf16x8& a, const bf16x8& b) {
    asm volatile("v_mfma_f32_16x16x32_bf16 %0, %1, %2, 0" : "=&v"(acc) : "v"(a), "v"(b));
}
template <int N> __device__ __forceinline__ void nop_on(f32x4& a, f32x4& b) { asm volatile("s_nop %2" : "+v"(a), "+v"(b) : "i"(N)); }
__device__ __forceinline__ void keep3(const bf16x8& a, const bf16x8& b, const bf16x8& c) { asm volatile("" :: "v"(a), "v"(b), "v"(c)); }
__device__ __forceinline__ int opaque_v(int v) { asm volatile("" : "+v"(v)); return v; }
__device__ __forceinline__ int opaque_s(int v) { asm volatile("" : "+s"(v)); return v; }
__device__ __forceinline__ void st16(u32x4 v, unsigned voff, __amdgpu_buffer_rsrc_t rs, unsigned soff) {
    asm volatile("buffer_store_dwordx4 %0, %1, %2, %3 offen" STORE_MOD "\n\ts_nop 1" :: "v"(v), "v"(voff), "s"(rs), "s"(soff) : "memory");
}
template <int N> __device__ __forceinline__ void waitq(u32x4 (&C)[8]) {
    asm volatile("s_waitcnt vmcnt(%8)" : "+v"(C[0]), "+v"(C[1]), "+v"(C[2]), "+v"(C[3]), "+v"(C[4]), "+v"(C[5]), "+v"(C[6]), "+v"(C[7]) : "i"(N > 63 ? 63 : N) : "memory");
}
template <int N> __device__ __forceinline__ void waitq2(u32x4& a, u32x4& b) {
    asm volatile("s_waitcnt vmcnt(%2)" : "+v"(a), "+v"(b) : "i"(N > 63 ? 63 : N) : "memory");
}
template <int N> __device__ __forceinline__ void waitv() { asm volatile("s_waitcnt vmcnt(%0)" :: "i"(N > 63 ? 63 : N) : "memory"); }
template <int N> __device__ __forceinline__ void waitl() { asm volatile("s_waitcnt lgkmcnt(%0)" :: "i"(N) : "memory"); }

#define STAMP(i)
#define STAMP_FLUSH()

#define STAMPC(i)
template <int S> struct IC { static constexpr int v = S; };
template <int I, int E, class F> __device__ __forceinline__ void sfor(F&& f) { if constexpr (I < E) { f(IC<I>{}); sfor<I + 1, E>(f); } }

__global__ __launch_bounds__(512, 2) void sq_kernel(const char* __restrict__ x, const char* __restrict__ qw,
                                                    const char* __restrict__ lut, char* __restrict__ out, unsigned long long* ws) {
    extern __shared__ __attribute__((aligned(16))) char smem[];
    lds_char* L = (lds_char*)smem;
    const int b = blockIdx.x, xcd = b & 7, idx = b >> 3;
    const int item = (xcd < 4 ? 32 * xcd : 128 + 31 * (xcd - 4)) + idx;
    const int strip = item / 7, part = item - strip * 7;
    const int nrt = 18 + (part < 2), rt0 = part * 18 + (part < 2 ? part : 2);
    const int koff = (part + 2 * strip) % 9;
    const int roff = (strip * 4) % nrt;
    const int tid = threadIdx.x, lane = tid & 63;
    const int wave = __builtin_amdgcn_readfirstlane(tid >> 6);
    auto gtile = [&](int p) { int t = p + roff; t = t >= nrt ? t - nrt : t; t = t >= nrt ? t - nrt : t; return rt0 + t; };
    auto kstage = [&](int p) { int t = p + koff; return t >= 9 ? t - 9 : t; };
    auto gtile_s = [&](int p) { int t = p + opaque_s(roff); t = t >= nrt ? t - nrt : t; t = t >= nrt ? t - nrt : t; return rt0 + t; };
    auto kstage_s = [&](int p) { int t = p + opaque_s(koff); return t >= 9 ? t - 9 : t; };
    STAMP(0);

    if (wave < 4) {
        const int c = wave;
        {
            const int code = tid >> 4, g8 = tid & 15;
            u32x4 v = *(const u32x4*)(lut + (size_t)(code * N_ + strip * 128 + g8 * 8) * 2);
            lds_char* p = L + LT + code * 512 + g8 * 8;
            *(__attribute__((address_space(3))) u32x2*)(p + 0)   = u32x2{v[0] & 0xffffu, v[2] & 0xffffu};
            *(__attribute__((address_space(3))) u32x2*)(p + 128) = u32x2{v[0] >> 16, v[2] >> 16};
            *(__attribute__((address_space(3))) u32x2*)(p + 256) = u32x2{v[1] & 0xffffu, v[3] & 0xffffu};
            *(__attribute__((address_space(3))) u32x2*)(p + 384) = u32x2{v[1] >> 16, v[3] >> 16};
        }
        BAR_LGKM();
        STAMPC(1);
        const int j = lane & 15, q = lane >> 4;
        const int fo0 = j * 128 + (((0 + q) ^ ((j >> 1) & 7)) << 4);
        const int fo1 = j * 128 + (((4 + q) ^ ((j >> 1) & 7)) << 4);
        const int fh0 = opaque_v(fo0 + 3 * SLOT), fh1 = opaque_v(fo1 + 3 * SLOT);
        const int wb0 = opaque_v(WB + c * 4096 + fo0), wb1 = opaque_v(WB + c * 4096 + fo1);
        const int wb0x = opaque_v(WB + c * 4096 + (fo0 ^ 16)), wb1x = opaque_v(WB + c * 4096 + (fo1 ^ 16));
        const int oo = j * 64 + ((q ^ ((j >> 1) & 3)) << 4);
        auto rd = [&](int off) { return *(const __attribute__((address_space(3))) bf16x8*)(L + off); };
        const int ow = opaque_v(WB + c * 4096 + oo), oz = opaque_v(OS + c * 1024 + oo);
        auto rdx = [&](int slot, int f) {
            const int base = slot < 3 ? ((f & 1) ? fo1 : fo0) : ((f & 1) ? fh1 : fh0);
            return rd(base + XR + (slot < 3 ? slot : slot - 3) * SLOT + (f >> 1) * 2048);
        };
        auto wr_out = [&](int addr, const f32x4& a0, const f32x4& a1) {
            u32x4 v = {cvtpk(a0[0], a0[1]), cvtpk(a0[2], a0[3]), cvtpk(a1[0], a1[1]), cvtpk(a1[2], a1[3])};
            *(__attribute__((address_space(3))) u32x4*)(L + addr) = v;
        };
        bf16x8 W[18][2];
        f32x4 acc[9][2];
        BAR();
        STAMPC(2);
        constexpr int PF = 6;
        bf16x8 xf[PF];
        sfor<0, 9>([&](auto U) {
            constexpr int u = U.v;
            W[2 * u][0] = rd(wb0 + (u & 1) * 16384);
            W[2 * u][1] = rd(wb0x + (u & 1) * 16384 + 2048);
            constexpr int sl = u % NSLOT;
            constexpr int P1 = 4;
            bf16x8 yf[P1];
            yf[0] = rdx(sl, 0);
            W[2 * u + 1][0] = rd(wb1 + (u & 1) * 16384);
            W[2 * u + 1][1] = rd(wb1x + (u & 1) * 16384 + 2048);
#pragma unroll
            for (int p = 1; p < P1; ++p) yf[p] = rdx(sl, p);
            __builtin_amdgcn_sched_barrier(0);
#pragma unroll
            for (int f = 0; f < 18; ++f) {
                if (u == 0 && !(f & 1)) {
                    mfma_zero(acc[f >> 1][0], W[2 * u + (f & 1)][0], yf[f % P1]);
                    mfma_zero(acc[f >> 1][1], W[2 * u + (f & 1)][1], yf[f % P1]);
                } else {
                    mfma_acc(acc[f >> 1][0], W[2 * u + (f & 1)][0], yf[f % P1]);
                    mfma_acc(acc[f >> 1][1], W[2 * u + (f & 1)][1], yf[f % P1]);
                }
                if (f + P1 < 18) yf[f % P1] = rdx(sl, f + P1);
                if (u == 8 && f >= 5 && (f & 1)) { const int i = (f - 5) >> 1; wr_out(ow + (i >> 2) * 16384 + (i & 3) * 1024, acc[i][0], acc[i][1]); }
                __builtin_amdgcn_sched_barrier(0);
            }
            if constexpr (u == 8) {
#pragma unroll
                for (int p = 0; p < PF; ++p) xf[p] = rdx(9 % NSLOT, p);
                __builtin_amdgcn_sched_barrier(0);
                nop_on<3>(acc[7][0], acc[7][1]);
                wr_out(ow + 16384 + 3 * 1024, acc[7][0], acc[7][1]);
                nop_on<3>(acc[8][0], acc[8][1]);
                wr_out(oz + 8192, acc[8][0], acc[8][1]);
                BAR_LGKM();
            } else {
                BAR();
            }
            STAMPC(3 + u);
        });
        f32x4 a2[2][2];
        sfor<9, NU>([&](auto U) {
            constexpr int u = U.v;
            constexpr int sl = u % NSLOT, nl = (u + 1) % NSLOT;
#pragma unroll
            for (int f = 0; f < 18; ++f) {
                if (f == 0) {
                    mfma_zero(a2[u & 1][0], W[f][0], xf[f % PF]);
                    mfma_zero(a2[u & 1][1], W[f][1], xf[f % PF]);
                } else {
                    mfma_acc(a2[u & 1][0], W[f][0], xf[f % PF]);
                    mfma_acc(a2[u & 1][1], W[f][1], xf[f % PF]);
                }
                if (f + PF < 18) xf[f % PF] = rdx(sl, f + PF);
                else if (u + 1 < NU) xf[f % PF] = rdx(nl, f + PF - 18);
                if (f == 3 && u > 9) wr_out(oz + ((u - 1) & 1) * 4096, a2[(u - 1) & 1][0], a2[(u - 1) & 1][1]);
                __builtin_amdgcn_sched_barrier(0);
            }
            if constexpr (u == NU - 1) {
                nop_on<8>(a2[u & 1][0], a2[u & 1][1]);
                wr_out(oz + 8192, a2[u & 1][0], a2[u & 1][1]);
                BAR_LGKM();
            } else {
                BAR();
            }
            STAMPC(3 + u);
        });
    } else {
        const int h = wave - 4;
        const __amdgpu_buffer_rsrc_t rsx = __builtin_amdgcn_make_buffer_rsrc((void*)x, (short)0, M_ * K_ * 2, 0x00020000);
        const __amdgpu_buffer_rsrc_t rsq = __builtin_amdgcn_make_buffer_rsrc((void*)qw, (short)0, K_ * N_ * 4, 0x00020000);
        const __amdgpu_buffer_rsrc_t rso = __builtin_amdgcn_make_buffer_rsrc((void*)out, (short)0, M_ * N_ * 2, 0x00020000);
        const int cq = lane & 31, g2 = lane >> 5, g = 2 * h + g2;
        unsigned vq[8];
#pragma unroll
        for (int i = 0; i < 8; ++i) vq[i] = (unsigned)((g * 8 + i) * (N_ * 4) + strip * 512 + cq * 16);
        u32x4 Q[QD][8];
        const unsigned m0b = (unsigned)(h * 4608);
        unsigned vx1[5], vx2[5];
#pragma unroll
        for (int i = 0; i < 5; ++i) {
            int rho = 36 * h + 8 * i + (lane >> 3);
            if (rho > 143) rho = 143;
            const int piece = rho >> 4, r = rho & 15, sc = (lane & 7) ^ ((r >> 1) & 7);
            vx1[i] = (unsigned)((gtile(piece) * 16 + r) * (K_ * 2) + sc * 16);
            vx2[i] = (unsigned)(r * (K_ * 2) + kstage(piece) * 128 + sc * 16);
        }
        const int srow = 4 * h + (lane >> 4), scc = lane & 15;
        const int so_in = srow * 64 + (((scc & 3) ^ ((srow >> 1) & 3)) << 4);
        const int sa_wb = opaque_v(WB + (scc >> 2) * 4096 + so_in), sa_os = opaque_v(OS + (scc >> 2) * 1024 + so_in);
        const unsigned vo = (unsigned)(srow * (N_ * 2) + strip * 256 + scc * 16);
        auto tile_lds = [&](int p) { return p < 8 ? sa_wb + (p >> 2) * 16384 + (p & 3) * 1024 : (p == 8 || p == NU - 1) ? sa_os + 8192 : sa_os + (p & 1) * 4096; };
        auto rd_tile = [&](int p) { return *(const __attribute__((address_space(3))) u32x4*)(L + tile_lds(p)); };
        u32x4 SV[MAXO];
        unsigned soq[9], sox[NU];
        auto run_ops = [&](auto S, auto Pp) {
            constexpr int s = S.v, p = Pp.v;
            sfor<0, PR.n[s + 2][p]>([&](auto Kk) {
                constexpr Op o = PR.op[s + 2][p][Kk.v];
                if constexpr (o.type == OP_Q) {
                    if constexpr (o.sub == 0) soq[o.idx] = (unsigned)(kstage_s(o.idx) * 64 * (N_ * 4));
                    qload(Q[o.idx % QD][o.sub], vq[o.sub], rsq, soq[o.idx]);
                } else if constexpr (o.type == OP_X) {
                    constexpr int u = o.idx, i = o.sub;
                    if constexpr (i == 0) sox[u] = u < 9 ? (unsigned)(kstage_s(u) * 128) : (unsigned)(gtile_s(u) * SLOT);
                    constexpr int sb = XR + (u % NSLOT) * SLOT + i * 1024;
                    if constexpr (i < 4) dma16<sb>(m0b, u < 9 ? vx1[i] : vx2[i], rsx, sox[u]);
                    else dma16h<sb>(m0b, u < 9 ? vx1[i] : vx2[i], rsx, sox[u]);
                } else if constexpr (o.type == OP_S) {
                    st16(SV[Kk.v], vo, rso, (unsigned)(gtile_s(o.idx) * 16 * N_ * 2));
                }
            });
            __builtin_amdgcn_sched_barrier(0);
        };
        sfor<0, 5>([&](auto Pp) { run_ops(IC<-2>{}, Pp); });
        STAMP(1);
        BAR();
        const int m = (cq >> 1) & 3, t = cq & 1;
        const int wa = WB + (cq >> 3) * 4096 + t * 2048 + m * 512;
        const int wa0 = opaque_v(wa + ((g ^ t ^ (2 * m)) << 4)), wa1 = opaque_v(wa + ((g ^ t ^ (2 * m + 1)) << 4));
        const int ltb = opaque_v(LT + cq * 4);
        sfor<-1, NU>([&](auto S) {
            constexpr int s = S.v;
            if constexpr (s + 1 < 9) {
                constexpr int st = s + 1;
                u32x4 (&C)[8] = Q[st % QD];
                unsigned lk[4][8];
                u32x4 w[4];
                sfor<0, 5>([&](auto Pp) {
                    constexpr int p = Pp.v;
                    if constexpr (p < 4) {
                        waitq2<PR.cum[s + 2][p] - PR.qpos[st][2 * p + 1]>(C[2 * p], C[2 * p + 1]);
#pragma unroll
                        for (int e = 0; e < 4; ++e) {
                            lk[p][2 * e] = *(const __attribute__((address_space(3))) unsigned*)(L + ltb + (C[2 * p][e] << 9) + e * 128);
                            lk[p][2 * e + 1] = *(const __attribute__((address_space(3))) unsigned*)(L + ltb + (C[2 * p + 1][e] << 9) + e * 128);
                        }
                        __builtin_amdgcn_sched_barrier(0);
                    }
                    if constexpr (p > 0) {
#pragma unroll
                        for (int e = 0; e < 4; ++e) w[e][p - 1] = lk[p - 1][2 * e] | (lk[p - 1][2 * e + 1] << 16);
                    }
                    if constexpr (p == 4) {
#pragma unroll
                        for (int e = 0; e < 4; ++e)
                            *(__attribute__((address_space(3))) u32x4*)(L + (e < 2 ? wa0 : wa1) + (st & 1) * 16384 + e * 128) = w[e];
                    }
                    __builtin_amdgcn_sched_barrier(0);
                    run_ops(S, Pp);
                });
            } else {
                sfor<0, PR.n[s + 2][1]>([&](auto Kk) { SV[Kk.v] = rd_tile(PR.op[s + 2][1][Kk.v].idx); });
                __builtin_amdgcn_sched_barrier(0);
                run_ops(S, IC<0>{});
                run_ops(S, IC<1>{});
            }
            constexpr int need0 = (s >= 7) ? s + 2 : s + 1;
            constexpr int need = need0 > NU - 1 ? NU - 1 : need0;
            waitv<PR.cumend[s + 2] - PR.xpos[need]>();
            BAR_LGKM();
            STAMP(3 + s);
        });
        {
            u32x4 v0 = rd_tile(NU - 2), v1 = rd_tile(NU - 1);
            st16(v0, vo, rso, (unsigned)(gtile_s(NU - 2) * 16 * N_ * 2));
            if (nrt == NU) st16(v1, vo, rso, (unsigned)(gtile_s(NU - 1) * 16 * N_ * 2));
        }
    }
}

extern "C" void kernel_launch(void* const* d_in, const int* in_sizes, int n_in,
                              void* d_out, int out_size, void* d_ws, size_t ws_size,
                              hipStream_t stream) {
    (void)hipFuncSetAttribute((const void*)sq_kernel, hipFuncAttributeMaxDynamicSharedMemorySize, LDS_BYTES);
    sq_kernel<<<252, 512, LDS_BYTES, stream>>>((const char*)d_in[0], (const char*)d_in[1], (const char*)d_in[2], (char*)d_out, (unsigned long long*)d_ws);
}
```

```cpp
#include <hip/hip_runtime.h>
#include <hip/hip_bf16.h>
typedef short bf16x8 __attribute__((ext_vector_type(8)));
typedef float f32x4 __attribute__((ext_vector_type(4)));
typedef unsigned u32x4 __attribute__((ext_vector_type(4)));
typedef unsigned u32x2 __attribute__((ext_vector_type(2)));
typedef __attribute__((address_space(3))) char lds_char;

constexpr int M_ = 2048, K_ = 576, N_ = 4608;
constexpr int SLOT = 18432, NSLOT = 6;
constexpr int XR = 0, WB = NSLOT * SLOT, LT = WB + 32768, OS = LT + 8192, LDS_BYTES = OS + 12288;
constexpr int NU = 19;
#define QD 2
#define STORE_MOD " sc1"
#define SQ1 3

enum { OP_NONE = 0, OP_Q = 1, OP_X = 2, OP_S = 3 };
struct Op { int type, idx, sub; };
constexpr int MAXO = 24;
struct Prog {
    Op op[NU + 2][5][MAXO];
    int n[NU + 2][5];
    int cum[NU + 2][5];
    int cumend[NU + 2];
    int qpos[9][8];
    int xpos[NU];
};
constexpr int xfirst(int s) {
    if (s <= -2) return 0;
    if (s <= 8 - QD) return s + 2;
    int base = 8 - QD + 2, v = base + (s - (8 - QD)) * 3;
    int cap = s + 5;
    if (v > cap) v = cap;
    if (v > NU) v = NU;
    return v;
}
constexpr Prog make_prog() {
    Prog P{};
    auto add = [&](int s, int p, Op o) { P.op[s + 2][p][P.n[s + 2][p]++] = o; };
    for (int i = 0; i < 8; ++i) add(-2, 0, Op{OP_Q, 0, i});
    for (int i = 0; i < 5; ++i) add(-2, 1, Op{OP_X, 0, i});
    for (int st = 1; st < QD; ++st) for (int i = 0; i < 8; ++i) add(-2, 2 + (st - 1), Op{OP_Q, st, i});
    for (int s = -1; s < NU; ++s) {
        const int qs = s + 1 + QD;
        for (int p = 0; p < 5; ++p) {
            if (qs < 9 && p < 4) { add(s, p, Op{OP_Q, qs, 2 * p}); add(s, p, Op{OP_Q, qs, 2 * p + 1}); }
        }
        int u0 = xfirst(s), u1 = xfirst(s + 1);
        int tot = (u1 - u0) * 5, k = 0;
        for (int u = u0; u < u1; ++u) for (int i = 0; i < 5; ++i, ++k) add(s, s + 1 < 9 ? k * 5 / tot : 0, Op{OP_X, u, i});
        if (s >= 9) { for (int i = (s - 9) * SQ1; i < (s - 8) * SQ1 && i < 9; ++i) add(s, 1, Op{OP_S, i, 0}); }
        if (s >= 11) add(s, 1, Op{OP_S, s - 2, 0});
    }
    int c = 0;
    for (int s = 0; s < NU + 2; ++s) {
        for (int p = 0; p < 5; ++p) {
            P.cum[s][p] = c;
            for (int k = 0; k < P.n[s][p]; ++k) {
                ++c;
                const Op o = P.op[s][p][k];
                if (o.type == OP_Q) P.qpos[o.idx][o.sub] = c;
                if (o.type == OP_X) P.xpos[o.idx] = c;
            }
        }
        P.cumend[s] = c;
    }
    return P;
}
constexpr Prog PR = make_prog();

#define BAR() do { __builtin_amdgcn_sched_barrier(0); asm volatile("s_barrier" ::: "memory"); __builtin_amdgcn_sched_barrier(0); } while (0)
#define BAR_LGKM() do { __builtin_amdgcn_sched_barrier(0); asm volatile("s_waitcnt lgkmcnt(0)\n\ts_barrier" ::: "memory"); __builtin_amdgcn_sched_barrier(0); } while (0)

__device__ __forceinline__ unsigned cvtpk(float a, float b) {
    typedef __bf16 bf2 __attribute__((ext_vector_type(2)));
    typedef float f2 __attribute__((ext_vector_type(2)));
    f2 v = {a, b};
    bf2 r = __builtin_convertvector(v, bf2);
    return __builtin_bit_cast(unsigned, r);
}

__device__ __forceinline__ void qload(u32x4& dst, unsigned voff, __amdgpu_buffer_rsrc_t rs, unsigned soff) {
    asm volatile("buffer_load_dwordx4 %0, %1, %2, %3 offen" : "=v"(dst) : "v"(voff), "s"(rs), "s"(soff) : "memory");
}
template <int IMM> __device__ __forceinline__ void dma16(unsigned m0v, unsigned voff, __amdgpu_buffer_rsrc_t rs, unsigned soff) {
    asm volatile("s_add_u32 m0, %0, %4\n\ts_nop 0\n\tbuffer_load_dwordx4 %1, %2, %3 offen lds" :: "s"(m0v), "v"(voff), "s"(rs), "s"(soff), "i"(IMM) : "memory", "scc");
}
template <int IMM> __device__ __forceinline__ void dma16h(unsigned m0v, unsigned voff, __amdgpu_buffer_rsrc_t rs, unsigned soff) {
    asm volatile("s_add_u32 m0, %0, %4\n\ts_mov_b32 exec_hi, 0\n\tbuffer_load_dwordx4 %1, %2, %3 offen lds\n\ts_mov_b32 exec_hi, -1" :: "s"(m0v), "v"(voff), "s"(rs), "s"(soff), "i"(IMM) : "memory", "scc");
}
__device__ __forceinline__ void mfma_acc(f32x4& acc, const bf16x8& a, const bf16x8& b) {
    asm volatile("v_mfma_f32_16x16x32_bf16 %0, %1, %2, %0" : "+v"(acc) : "v"(a), "v"(b));
}
__device__ __forceinline__ void mfma_zero(f32x4& acc, const bf16x8& a, const bf16x8& b) {
    asm volatile("v_mfma_f32_16x16x32_bf16 %0, %1, %2, 0" : "=&v"(acc) : "v"(a), "v"(b));
}
template <int N> __device__ __forceinline__ void nop_on(f32x4& a, f32x4& b) { asm volatile("s_nop %2" : "+v"(a), "+v"(b) : "i"(N)); }
__device__ __forceinline__ void keep3(const bf16x8& a, const bf16x8& b, const bf16x8& c) { asm volatile("" :: "v"(a), "v"(b), "v"(c)); }
__device__ __forceinline__ int opaque_v(int v) { asm volatile("" : "+v"(v)); return v; }
__device__ __forceinline__ int opaque_s(int v) { asm volatile("" : "+s"(v)); return v; }
__device__ __forceinline__ void st16(u32x4 v, unsigned voff, __amdgpu_buffer_rsrc_t rs, unsigned soff) {
    asm volatile("buffer_store_dwordx4 %0, %1, %2, %3 offen" STORE_MOD "\n\ts_nop 1" :: "v"(v), "v"(voff), "s"(rs), "s"(soff) : "memory");
}
template <int N> __device__ __forceinline__ void waitq(u32x4 (&C)[8]) {
    asm volatile("s_waitcnt vmcnt(%8)" : "+v"(C[0]), "+v"(C[1]), "+v"(C[2]), "+v"(C[3]), "+v"(C[4]), "+v"(C[5]), "+v"(C[6]), "+v"(C[7]) : "i"(N > 63 ? 63 : N) : "memory");
}
template <int N> __device__ __forceinline__ void waitq2(u32x4& a, u32x4& b) {
    asm volatile("s_waitcnt vmcnt(%2)" : "+v"(a), "+v"(b) : "i"(N > 63 ? 63 : N) : "memory");
}
template <int N> __device__ __forceinline__ void waitv() { asm volatile("s_waitcnt vmcnt(%0)" :: "i"(N > 63 ? 63 : N) : "memory"); }
template <int N> __device__ __forceinline__ void waitl() { asm volatile("s_waitcnt lgkmcnt(%0)" :: "i"(N) : "memory"); }

#define AMP 0
#define ROT 1
__device__ __forceinline__ unsigned long long stamp_now() { unsigned long long t_; asm volatile("s_memtime %0\n\ts_waitcnt lgkmcnt(0)" : "=s"(t_) :: "memory"); return t_; }
#define STAMPS
#define STAMP(i)
#define PSTAMP(i) do { unsigned tt_ = (unsigned)(stamp_now() - tbase); stampv = ((i) == AMP) ? tt_ : stampv; } while (0)
#define STAMP_FLUSH() do { unsigned long long te_ = stamp_now(); while (stamp_now() - te_ < 4ull * stampv) __builtin_amdgcn_s_sleep(1); } while (0)
template <int IMM> __device__ __forceinline__ void dma16p(unsigned m0v, unsigned voff, __amdgpu_buffer_rsrc_t rs, unsigned& soff, unsigned step) {
    asm volatile("s_add_u32 m0, %1, %5\n\ts_nop 0\n\tbuffer_load_dwordx4 %2, %3, %0 offen lds\n\ts_add_u32 %0, %0, %4"
                 : "+s"(soff) : "s"(m0v), "v"(voff), "s"(rs), "s"(step), "i"(IMM) : "memory", "scc");
}

#define STAMPC(i)
template <int S> struct IC { static constexpr int v = S; };
template <int I, int E, class F> __device__ __forceinline__ void sfor(F&& f) { if constexpr (I < E) { f(IC<I>{}); sfor<I + 1, E>(f); } }

__global__ __launch_bounds__(512, 2) void sq_kernel(const char* __restrict__ x, const char* __restrict__ qw,
                                                    const char* __restrict__ lut, char* __restrict__ out, unsigned long long* ws) {
    extern __shared__ __attribute__((aligned(16))) char smem[];
    lds_char* L = (lds_char*)smem;
    const int b = blockIdx.x, xcd = b & 7, idx = b >> 3;
    const int item = (xcd < 4 ? 32 * xcd : 128 + 31 * (xcd - 4)) + idx;
    const int strip = item / 7, part = item - strip * 7;
    const int nrt = 18 + (part < 2), rt0 = part * 18 + (part < 2 ? part : 2);
    const int koff = (part + 2 * strip) % 9;
    const int roff = (strip * 4) % nrt;
    const int tid = threadIdx.x, lane = tid & 63;
    const int wave = __builtin_amdgcn_readfirstlane(tid >> 6);
    auto gtile = [&](int p) { int t = p + roff; t = t >= nrt ? t - nrt : t; t = t >= nrt ? t - nrt : t; return rt0 + t; };
    auto kstage = [&](int p) { int t = p + koff; return t >= 9 ? t - 9 : t; };
    auto gtile_s = [&](int p) { int t = p + opaque_s(roff); t = t >= nrt ? t - nrt : t; t = t >= nrt ? t - nrt : t; return rt0 + t; };
    auto kstage_s = [&](int p) { int t = p + opaque_s(koff); return t >= 9 ? t - 9 : t; };
    const unsigned long long tbase = __builtin_amdgcn_s_memtime();
    unsigned stampv = 0;

    if (wave >= 4) {
        const unsigned h = wave - 4;
        const __amdgpu_buffer_rsrc_t rsq = __builtin_amdgcn_make_buffer_rsrc((void*)qw, (short)0, K_ * N_ * 4, 0x00020000);
        const __amdgpu_buffer_rsrc_t rsx = __builtin_amdgcn_make_buffer_rsrc((void*)x, (short)0, M_ * K_ * 2, 0x00020000);
        const int cq = lane & 31, g2 = lane >> 5, g = 2 * h + g2;
        unsigned vq[8];
#pragma unroll
        for (int i = 0; i < 8; ++i) vq[i] = (unsigned)((g * 8 + i) * (N_ * 4) + cq * 16 + strip * 512);
        const int xr = (h & 1) * 8 + (lane >> 3);
        const unsigned vx = (unsigned)(xr * (K_ * 2) + (((lane & 7) ^ ((xr >> 1) & 7)) << 4));
        unsigned kof = ROT == 0 ? 0 : (part + 2 * strip) % 9;
        u32x4 Q[8];
        unsigned xm = h * 1024;
        PSTAMP(0);
#pragma nounroll
        for (unsigned s = 0; s < 19; ++s) {
            unsigned ks = s + kof; ks = ks >= 9 ? ks - 9 : ks;
            if (s < 9) {
#pragma unroll
                for (int i = 0; i < 8; ++i) qload(Q[i], vq[i], rsq, ks * 64 * N_ * 4);
            }
            unsigned tl = rt0 + s; tl = tl > 127 ? 127 : tl;
            unsigned so = s < 9 ? (rt0 + (h >> 1)) * SLOT + ks * 128 : tl * SLOT + (h >> 1) * 128;
            unsigned xstep = s < 9 ? 2 * SLOT : 256;
            dma16p<0>(xm, vx, rsx, so, xstep); dma16p<4096>(xm, vx, rsx, so, xstep);
            dma16p<8192>(xm, vx, rsx, so, xstep); dma16p<12288>(xm, vx, rsx, so, xstep);
            if (h < 2) dma16p<16384>(xm, vx, rsx, so, xstep);
            xm = (xm >= 5 * SLOT) ? xm - 5 * SLOT : xm + SLOT;
            asm volatile("s_waitcnt vmcnt(24)" ::: "memory");
            PSTAMP(s + 1);
        }
        asm volatile("s_waitcnt vmcnt(0)" ::: "memory");
        PSTAMP(20);
#pragma unroll
        for (int i = 0; i < 8; ++i) asm volatile("" :: "v"(Q[i]));
    }
    BAR();

    if (wave < 4) {
        const int c = wave;
        {
            const int code = tid >> 4, g8 = tid & 15;
            u32x4 v = *(const u32x4*)(lut + (size_t)(code * N_ + strip * 128 + g8 * 8) * 2);
            lds_char* p = L + LT + code * 512 + g8 * 8;
            *(__attribute__((address_space(3))) u32x2*)(p + 0)   = u32x2{v[0] & 0xffffu, v[2] & 0xffffu};
            *(__attribute__((address_space(3))) u32x2*)(p + 128) = u32x2{v[0] >> 16, v[2] >> 16};
            *(__attribute__((address_space(3))) u32x2*)(p + 256) = u32x2{v[1] & 0xffffu, v[3] & 0xffffu};
            *(__attribute__((address_space(3))) u32x2*)(p + 384) = u32x2{v[1] >> 16, v[3] >> 16};
        }
        BAR_LGKM();
        STAMPC(1);
        const int j = lane & 15, q = lane >> 4;
        const int fo0 = j * 128 + (((0 + q) ^ ((j >> 1) & 7)) << 4);
        const int fo1 = j * 128 + (((4 + q) ^ ((j >> 1) & 7)) << 4);
        const int fh0 = opaque_v(fo0 + 3 * SLOT), fh1 = opaque_v(fo1 + 3 * SLOT);
        const int wb0 = opaque_v(WB + c * 4096 + fo0), wb1 = opaque_v(WB + c * 4096 + fo1);
        const int wb0x = opaque_v(WB + c * 4096 + (fo0 ^ 16)), wb1x = opaque_v(WB + c * 4096 + (fo1 ^ 16));
        const int oo = j * 64 + ((q ^ ((j >> 1) & 3)) << 4);
        auto rd = [&](int off) { return *(const __attribute__((address_space(3))) bf16x8*)(L + off); };
        const int ow = opaque_v(WB + c * 4096 + oo), oz = opaque_v(OS + c * 1024 + oo);
        auto rdx = [&](int slot, int f) {
            const int base = slot < 3 ? ((f & 1) ? fo1 : fo0) : ((f & 1) ? fh1 : fh0);
            return rd(base + XR + (slot < 3 ? slot : slot - 3) * SLOT + (f >> 1) * 2048);
        };
        auto wr_out = [&](int addr, const f32x4& a0, const f32x4& a1) {
            u32x4 v = {cvtpk(a0[0], a0[1]), cvtpk(a0[2], a0[3]), cvtpk(a1[0], a1[1]), cvtpk(a1[2], a1[3])};
            *(__attribute__((address_space(3))) u32x4*)(L + addr) = v;
        };
        bf16x8 W[18][2];
        f32x4 acc[9][2];
        BAR();
        STAMPC(2);
        constexpr int PF = 6;
        bf16x8 xf[PF];
        sfor<0, 9>([&](auto U) {
            constexpr int u = U.v;
            W[2 * u][0] = rd(wb0 + (u & 1) * 16384);
            W[2 * u][1] = rd(wb0x + (u & 1) * 16384 + 2048);
            constexpr int sl = u % NSLOT;
            constexpr int P1 = 4;
            bf16x8 yf[P1];
            yf[0] = rdx(sl, 0);
            W[2 * u + 1][0] = rd(wb1 + (u & 1) * 16384);
            W[2 * u + 1][1] = rd(wb1x + (u & 1) * 16384 + 2048);
#pragma unroll
            for (int p = 1; p < P1; ++p) yf[p] = rdx(sl, p);
            __builtin_amdgcn_sched_barrier(0);
#pragma unroll
            for (int f = 0; f < 18; ++f) {
                if (u == 0 && !(f & 1)) {
                    mfma_zero(acc[f >> 1][0], W[2 * u + (f & 1)][0], yf[f % P1]);
                    mfma_zero(acc[f >> 1][1], W[2 * u + (f & 1)][1], yf[f % P1]);
                } else {
                    mfma_acc(acc[f >> 1][0], W[2 * u + (f & 1)][0], yf[f % P1]);
                    mfma_acc(acc[f >> 1][1], W[2 * u + (f & 1)][1], yf[f % P1]);
                }
                if (f + P1 < 18) yf[f % P1] = rdx(sl, f + P1);
                if (u == 8 && f >= 5 && (f & 1)) { const int i = (f - 5) >> 1; wr_out(ow + (i >> 2) * 16384 + (i & 3) * 1024, acc[i][0], acc[i][1]); }
                __builtin_amdgcn_sched_barrier(0);
            }
            if constexpr (u == 8) {
#pragma unroll
                for (int p = 0; p < PF; ++p) xf[p] = rdx(9 % NSLOT, p);
                __builtin_amdgcn_sched_barrier(0);
                nop_on<3>(acc[7][0], acc[7][1]);
                wr_out(ow + 16384 + 3 * 1024, acc[7][0], acc[7][1]);
                nop_on<3>(acc[8][0], acc[8][1]);
                wr_out(oz + 8192, acc[8][0], acc[8][1]);
                BAR_LGKM();
            } else {
                BAR();
            }
            STAMPC(3 + u);
        });
        f32x4 a2[2][2];
        sfor<9, NU>([&](auto U) {
            constexpr int u = U.v;
            constexpr int sl = u % NSLOT, nl = (u + 1) % NSLOT;
#pragma unroll
            for (int f = 0; f < 18; ++f) {
                if (f == 0) {
                    mfma_zero(a2[u & 1][0], W[f][0], xf[f % PF]);
                    mfma_zero(a2[u & 1][1], W[f][1], xf[f % PF]);
                } else {
                    mfma_acc(a2[u & 1][0], W[f][0], xf[f % PF]);
                    mfma_acc(a2[u & 1][1], W[f][1], xf[f % PF]);
                }
                if (f + PF < 18) xf[f % PF] = rdx(sl, f + PF);
                else if (u + 1 < NU) xf[f % PF] = rdx(nl, f + PF - 18);
                if (f == 3 && u > 9) wr_out(oz + ((u - 1) & 1) * 4096, a2[(u - 1) & 1][0], a2[(u - 1) & 1][1]);
                __builtin_amdgcn_sched_barrier(0);
            }
            if constexpr (u == NU - 1) {
                nop_on<8>(a2[u & 1][0], a2[u & 1][1]);
                wr_out(oz + 8192, a2[u & 1][0], a2[u & 1][1]);
                BAR_LGKM();
            } else {
                BAR();
            }
            STAMPC(3 + u);
        });
    } else {
        const int h = wave - 4;
        const __amdgpu_buffer_rsrc_t rsx = __builtin_amdgcn_make_buffer_rsrc((void*)x, (short)0, M_ * K_ * 2, 0x00020000);
        const __amdgpu_buffer_rsrc_t rsq = __builtin_amdgcn_make_buffer_rsrc((void*)qw, (short)0, K_ * N_ * 4, 0x00020000);
        const __amdgpu_buffer_rsrc_t rso = __builtin_amdgcn_make_buffer_rsrc((void*)out, (short)0, M_ * N_ * 2, 0x00020000);
        const int cq = lane & 31, g2 = lane >> 5, g = 2 * h + g2;
        unsigned vq[8];
#pragma unroll
        for (int i = 0; i < 8; ++i) vq[i] = (unsigned)((g * 8 + i) * (N_ * 4) + strip * 512 + cq * 16);
        u32x4 Q[QD][8];
        const unsigned m0b = (unsigned)(h * 4608);
        unsigned vx1[5], vx2[5];
#pragma unroll
        for (int i = 0; i < 5; ++i) {
            int rho = 36 * h + 8 * i + (lane >> 3);
            if (rho > 143) rho = 143;
            const int piece = rho >> 4, r = rho & 15, sc = (lane & 7) ^ ((r >> 1) & 7);
            vx1[i] = (unsigned)((gtile(piece) * 16 + r) * (K_ * 2) + sc * 16);
            vx2[i] = (unsigned)(r * (K_ * 2) + kstage(piece) * 128 + sc * 16);
        }
        const int srow = 4 * h + (lane >> 4), scc = lane & 15;
        const int so_in = srow * 64 + (((scc & 3) ^ ((srow >> 1) & 3)) << 4);
        const int sa_wb = opaque_v(WB + (scc >> 2) * 4096 + so_in), sa_os = opaque_v(OS + (scc >> 2) * 1024 + so_in);
        const unsigned vo = (unsigned)(srow * (N_ * 2) + strip * 256 + scc * 16);
        auto tile_lds = [&](int p) { return p < 8 ? sa_wb + (p >> 2) * 16384 + (p & 3) * 1024 : (p == 8 || p == NU - 1) ? sa_os + 8192 : sa_os + (p & 1) * 4096; };
        auto rd_tile = [&](int p) { return *(const __attribute__((address_space(3))) u32x4*)(L + tile_lds(p)); };
        u32x4 SV[MAXO];
        unsigned soq[9], sox[NU];
        auto run_ops = [&](auto S, auto Pp) {
            constexpr int s = S.v, p = Pp.v;
            sfor<0, PR.n[s + 2][p]>([&](auto Kk) {
                constexpr Op o = PR.op[s + 2][p][Kk.v];
                if constexpr (o.type == OP_Q) {
                    if constexpr (o.sub == 0) soq[o.idx] = (unsigned)(kstage_s(o.idx) * 64 * (N_ * 4));
                    qload(Q[o.idx % QD][o.sub], vq[o.sub], rsq, soq[o.idx]);
                } else if constexpr (o.type == OP_X) {
                    constexpr int u = o.idx, i = o.sub;
                    if constexpr (i == 0) sox[u] = u < 9 ? (unsigned)(kstage_s(u) * 128) : (unsigned)(gtile_s(u) * SLOT);
                    constexpr int sb = XR + (u % NSLOT) * SLOT + i * 1024;
                    if constexpr (i < 4) dma16<sb>(m0b, u < 9 ? vx1[i] : vx2[i], rsx, sox[u]);
                    else dma16h<sb>(m0b, u < 9 ? vx1[i] : vx2[i], rsx, sox[u]);
                } else if constexpr (o.type == OP_S) {
                    st16(SV[Kk.v], vo, rso, (unsigned)(gtile_s(o.idx) * 16 * N_ * 2));
                }
            });
            __builtin_amdgcn_sched_barrier(0);
        };
        sfor<0, 5>([&](auto Pp) { run_ops(IC<-2>{}, Pp); });
        STAMP(1);
        BAR();
        const int m = (cq >> 1) & 3, t = cq & 1;
        const int wa = WB + (cq >> 3) * 4096 + t * 2048 + m * 512;
        const int wa0 = opaque_v(wa + ((g ^ t ^ (2 * m)) << 4)), wa1 = opaque_v(wa + ((g ^ t ^ (2 * m + 1)) << 4));
        const int ltb = opaque_v(LT + cq * 4);
        sfor<-1, NU>([&](auto S) {
            constexpr int s = S.v;
            if constexpr (s + 1 < 9) {
                constexpr int st = s + 1;
                u32x4 (&C)[8] = Q[st % QD];
                unsigned lk[4][8];
                u32x4 w[4];
                sfor<0, 5>([&](auto Pp) {
                    constexpr int p = Pp.v;
                    if constexpr (p < 4) {
                        waitq2<PR.cum[s + 2][p] - PR.qpos[st][2 * p + 1]>(C[2 * p], C[2 * p + 1]);
#pragma unroll
                        for (int e = 0; e < 4; ++e) {
                            lk[p][2 * e] = *(const __attribute__((address_space(3))) unsigned*)(L + ltb + (C[2 * p][e] << 9) + e * 128);
                            lk[p][2 * e + 1] = *(const __attribute__((address_space(3))) unsigned*)(L + ltb + (C[2 * p + 1][e] << 9) + e * 128);
                        }
                        __builtin_amdgcn_sched_barrier(0);
                    }
                    if constexpr (p > 0) {
#pragma unroll
                        for (int e = 0; e < 4; ++e) w[e][p - 1] = lk[p - 1][2 * e] | (lk[p - 1][2 * e + 1] << 16);
                    }
                    if constexpr (p == 4) {
#pragma unroll
                        for (int e = 0; e < 4; ++e)
                            *(__attribute__((address_space(3))) u32x4*)(L + (e < 2 ? wa0 : wa1) + (st & 1) * 16384 + e * 128) = w[e];
                    }
                    __builtin_amdgcn_sched_barrier(0);
                    run_ops(S, Pp);
                });
            } else {
                sfor<0, PR.n[s + 2][1]>([&](auto Kk) { SV[Kk.v] = rd_tile(PR.op[s + 2][1][Kk.v].idx); });
                __builtin_amdgcn_sched_barrier(0);
                run_ops(S, IC<0>{});
                run_ops(S, IC<1>{});
            }
            constexpr int need0 = (s >= 7) ? s + 2 : s + 1;
            constexpr int need = need0 > NU - 1 ? NU - 1 : need0;
            waitv<PR.cumend[s + 2] - PR.xpos[need]>();
            BAR_LGKM();
            STAMP(3 + s);
        });
        {
            u32x4 v0 = rd_tile(NU - 2), v1 = rd_tile(NU - 1);
            st16(v0, vo, rso, (unsigned)(gtile_s(NU - 2) * 16 * N_ * 2));
            if (nrt == NU) st16(v1, vo, rso, (unsigned)(gtile_s(NU - 1) * 16 * N_ * 2));
        }
        asm volatile("s_waitcnt vmcnt(0)" ::: "memory");
        STAMP(3 + NU);
        STAMP_FLUSH();
    }
}

extern "C" void kernel_launch(void* const* d_in, const int* in_sizes, int n_in,
                              void* d_out, int out_size, void* d_ws, size_t ws_size,
                              hipStream_t stream) {
    (void)hipFuncSetAttribute((const void*)sq_kernel, hipFuncAttributeMaxDynamicSharedMemorySize, LDS_BYTES);
    sq_kernel<<<252, 512, LDS_BYTES, stream>>>((const char*)d_in[0], (const char*)d_in[1], (const char*)d_in[2], (char*)d_out, (unsigned long long*)d_ws);
}
```

```cpp
#include <hip/hip_runtime.h>
#include <hip/hip_bf16.h>
typedef short bf16x8 __attribute__((ext_vector_type(8)));
typedef float f32x4 __attribute__((ext_vector_type(4)));
typedef int i32x4 __attribute__((ext_vector_type(4)));
typedef unsigned int u32x2 __attribute__((ext_vector_type(2)));
constexpr int M = 2048, K = 576, N = 4608;

__global__ __launch_bounds__(256) void k(const unsigned short* __restrict__ x, const int* __restrict__ qw,
                                         const unsigned short* __restrict__ lut, unsigned short* __restrict__ out) {
    __shared__ unsigned short slut[16 * 64];
    unsigned long long t0_ = __builtin_amdgcn_s_memrealtime();
    int bn = blockIdx.x % (N / 64), bm = blockIdx.x / (N / 64);
    int n0 = bn * 64;
    for (int i = threadIdx.x; i < 16 * 64; i += 256) slut[i] = lut[(i >> 6) * N + n0 + (i & 63)];
    __syncthreads();
    int lane = threadIdx.x & 63, w = threadIdx.x >> 6;
    int j = lane & 15, q = lane >> 4;
    int m0 = bm * 256 + w * 64;
    f32x4 acc[4][4] = {};
    for (int k0 = 0; k0 < K; k0 += 32) {
        bf16x8 a[4];
        for (int r = 0; r < 4; ++r) a[r] = *(const bf16x8*)(x + (size_t)(m0 + r * 16 + j) * K + k0 + q * 8);
        bf16x8 b[4];
        for (int i = 0; i < 8; ++i) {
            i32x4 c = *(const i32x4*)(qw + (size_t)(k0 + q * 8 + i) * N + n0 + j * 4);
            for (int t = 0; t < 4; ++t) b[t][i] = (short)slut[(c[t] & 15) * 64 + j * 4 + t];
        }
        for (int r = 0; r < 4; ++r)
            for (int t = 0; t < 4; ++t)
                acc[r][t] = __builtin_amdgcn_mfma_f32_16x16x32_bf16(a[r], b[t], acc[r][t], 0, 0, 0);
    }
    for (int r = 0; r < 4; ++r)
        for (int i = 0; i < 4; ++i) {
            int row = m0 + r * 16 + q * 4 + i;
            __hip_bfloat16 v[4];
            for (int t = 0; t < 4; ++t) v[t] = __float2bfloat16(acc[r][t][i]);
            *(u32x2*)(out + (size_t)row * N + n0 + j * 4) = *(u32x2*)v;
        }
    asm volatile("s_waitcnt vmcnt(0)" ::: "memory");
    unsigned long long t1_ = __builtin_amdgcn_s_memrealtime();
    while (__builtin_amdgcn_s_memrealtime() - t1_ < 0ull * (t1_ - t0_)) __builtin_amdgcn_s_sleep(1);
}
extern "C" void kernel_launch(void* const* d_in, const int* in_sizes, int n_in,
                              void* d_out, int out_size, void* d_ws, size_t ws_size,
                              hipStream_t stream) {
    k<<<(N / 64) * (M / 256), 256, 0, stream>>>((const unsigned short*)d_in[0], (const int*)d_in[1],
                                                 (const unsigned short*)d_in[2], (unsigned short*)d_out);
}
```

```cpp
#include <hip/hip_runtime.h>
#include <hip/hip_bf16.h>

__global__ __launch_bounds__(512, 2) void sq_kernel(const void* x, const void* qw, const void* lut, void* out) {
    asm volatile(
        "s_getpc_b64 s[22:23]\n"
        ".Lsq_begin:\n"
        "v_lshlrev_b32 v2, 5, %2\n"
        "v_min_u32 v2, .Lsq_end-.Lsq_begin, v2\n"
        "s_load_dwordx2 s[8:9], %0, 8\n"
        "s_load_dwordx2 s[20:21], %0, 16\n"
        "s_load_dwordx2 s[4:5], %0, 0\n"
        "s_load_dwordx2 s[12:13], %0, 24\n"
        "global_load_dword v254, v2, s[22:23]\n"
        "s_and_b32 s24, %1, 7\n"
        "s_lshr_b32 s25, %1, 3\n"
        "s_mul_i32 s26, s24, 31\n"
        "s_min_u32 s24, s24, 4\n"
        "s_add_u32 s25, s25, s26\n"
        "s_add_u32 s25, s25, s24\n"
        "v_readfirstlane_b32 s28, %2\n"
        "s_mul_i32 s26, s25, 9363\n"
        "s_lshr_b32 s26, s26, 16\n"
        "s_mul_i32 s27, s26, 7\n"
        "s_sub_u32 s27, s25, s27\n"
        "s_lshr_b32 s28, s28, 6\n"
        "v_and_b32 v1, 63, %2\n"
        "s_cmp_ge_u32 s28, 4\n"
        "s_cbranch_scc1 .Lsq_c0\n"
        "s_add_u32 s24, s27, s26\n"
        "s_and_b32 s24, s24, 1\n"
        "s_mov_b32 s52, 0x76543210\n"
        "s_mov_b32 s53, 8\n"
        "s_cmp_eq_u32 s24, 1\n"
        "s_cselect_b32 s52, 0x67452301, s52\n"
        "s_cselect_b32 s53, 8, s53\n"
        "s_and_b32 s31, s52, 15\n"
        "v_lshrrev_b32 v2, 5, v1\n"
        "v_and_b32 v3, 31, v1\n"
        "v_lshl_add_u32 v2, s28, 1, v2\n"
        "s_mov_b32 s10, 10616832\n"
        "s_mov_b32 s11, 0x20000\n"
        "v_mul_u32_u24 v96, 147456, v2\n"
        "s_mul_i32 s32, s31, 1179648\n"
        "s_lshl_b32 s24, s26, 9\n"
        "v_lshl_add_u32 v96, v3, 4, v96\n"
        "v_add_u32 v96, s24, v96\n"
        "v_add_u32 v97, 18432, v96\n"
        "v_add_u32 v98, 36864, v96\n"
        "v_add_u32 v99, 55296, v96\n"
        "v_add_u32 v100, 73728, v96\n"
        "v_add_u32 v101, 92160, v96\n"
        "v_add_u32 v102, 110592, v96\n"
        "v_add_u32 v103, 129024, v96\n"
        "s_waitcnt lgkmcnt(0)\n"
        "buffer_load_dwordx4 v[128:131], v96, s[8:11], s32 offen \n"
        "buffer_load_dwordx4 v[132:135], v97, s[8:11], s32 offen \n"
        "buffer_load_dwordx4 v[136:139], v98, s[8:11], s32 offen \n"
        "buffer_load_dwordx4 v[140:143], v99, s[8:11], s32 offen \n"
        "buffer_load_dwordx4 v[144:147], v100, s[8:11], s32 offen \n"
        "buffer_load_dwordx4 v[148:151], v101, s[8:11], s32 offen \n"
        "buffer_load_dwordx4 v[152:155], v102, s[8:11], s32 offen \n"
        "buffer_load_dwordx4 v[156:159], v103, s[8:11], s32 offen \n"
        "s_branch .Lsq_h0\n"
        ".Lsq_c0:\n"
        "s_setprio 3\n"
        "s_sub_u32 s28, s28, 4\n"
        "v_lshrrev_b32 v10, 4, v1\n"
        "v_and_b32 v11, 15, v1\n"
        "v_mul_u32_u24 v12, 36864, v10\n"
        "v_lshl_add_u32 v12, v11, 2, v12\n"
        "s_lshl_b32 s24, s26, 8\n"
        "s_lshl_b32 s25, s28, 6\n"
        "s_add_u32 s24, s24, s25\n"
        "v_add_u32 v12, s24, v12\n"
        "v_add_u32 v13, 9216, v12\n"
        "v_add_u32 v14, 18432, v12\n"
        "v_add_u32 v15, 27648, v12\n"
        "s_waitcnt lgkmcnt(0)\n"
        "global_load_dword v32, v12, s[20:21]\n"
        "global_load_dword v33, v13, s[20:21]\n"
        "global_load_dword v34, v14, s[20:21]\n"
        "global_load_dword v35, v15, s[20:21]\n"
        "s_mul_i32 s24, %1, 9472\n"
        "v_and_b32 v16, 255, %2\n"
        "v_lshlrev_b32 v16, 7, v16\n"
        "v_min_u32 v16, 9344, v16\n"
        "v_add_u32 v16, s24, v16\n"
        "v_min_u32 v16, 2359168, v16\n"
        "s_cmp_gt_u32 s28, 1\n"
        "s_cbranch_scc1 .Lsq_npf\n"
        "global_load_dword v255, v16, s[4:5]\n"
        ".Lsq_npf:\n"
        "s_setprio 1\n"
        "s_branch .Lsq_compute\n"
        ".Lsq_h0:\n"
        "s_mov_b64 s[54:55], s[52:53]\n"
        "v_lshlrev_b32 v120, 9, v2\n"
        "v_lshl_add_u32 v120, v3, 4, v120\n"
        "v_add_u32 v120, 116736, v120\n"
        "s_mov_b32 s6, 2359296\n"
        "s_mov_b32 s7, 0x20000\n"
        "v_lshrrev_b32 v4, 3, v1\n"
        "v_lshl_add_u32 v4, s28, 3, v4\n"
        "v_lshrrev_b32 v5, 1, v4\n"
        "v_xor_b32 v5, v5, v1\n"
        "v_and_b32 v5, 7, v5\n"
        "v_mul_u32_u24 v119, 1152, v4\n"
        "v_lshl_add_u32 v119, v5, 4, v119\n"
        "s_mul_i32 s29, s27, 18\n"
        "s_min_u32 s24, s27, 2\n"
        "s_add_u32 s29, s29, s24\n"
        "s_mul_i32 s24, s29, 18432\n"
        "v_add_u32 v104, s24, v119\n"
        "v_add_u32 v105, 36864, v104\n"
        "v_add_u32 v106, 73728, v104\n"
        "v_add_u32 v107, 110592, v104\n"
        "v_add_u32 v108, 147456, v104\n"
        "v_add_u32 v109, 184320, v104\n"
        "v_add_u32 v110, 221184, v104\n"
        "v_add_u32 v111, 258048, v104\n"
        "v_add_u32 v112, 294912, v104\n"
        "v_add_u32 v113, 331776, v104\n"
        "s_lshr_b64 s[54:55], s[54:55], 4\n"
        "s_and_b32 s32, s54, 15\n"
        "s_mul_i32 s32, s32, 1179648\n"
        "buffer_load_dwordx4 v[40:43], v96, s[8:11], s32 offen \n"
        "buffer_load_dwordx4 v[44:47], v97, s[8:11], s32 offen \n"
        "buffer_load_dwordx4 v[48:51], v98, s[8:11], s32 offen \n"
        "buffer_load_dwordx4 v[52:55], v99, s[8:11], s32 offen \n"
        "buffer_load_dwordx4 v[56:59], v100, s[8:11], s32 offen \n"
        "buffer_load_dwordx4 v[60:63], v101, s[8:11], s32 offen \n"
        "buffer_load_dwordx4 v[64:67], v102, s[8:11], s32 offen \n"
        "buffer_load_dwordx4 v[68:71], v103, s[8:11], s32 offen \n"
        "s_lshr_b64 s[54:55], s[54:55], 4\n"
        "s_and_b32 s32, s54, 15\n"
        "s_mul_i32 s32, s32, 1179648\n"
        "s_lshl_b32 s40, s31, 7\n"
        "s_bfe_u32 s41, s52, 0x40004\n"
        "s_lshl_b32 s41, s41, 7\n"
        "s_lshl_b32 s42, s53, 7\n"
        "s_lshr_b32 s52, s52, 8\n"
        "s_and_b32 s33, s52, 15\n"
        "s_lshl_b32 s33, s33, 7\n"
        "s_add_u32 s43, s41, 18432\n"
        "s_sub_u32 s44, s41, 18432\n"
        "s_cmp_ge_u32 s28, 2\n"
        "s_cselect_b32 s45, s44, s40\n"
        "s_cselect_b32 s46, s40, s42\n"
        "s_cselect_b32 s47, s42, s43\n"
        "v_add_u32 v116, s45, v104\n"
        "v_add_u32 v117, s46, v104\n"
        "v_add_u32 v118, s47, v104\n"
        "s_lshl_b32 s39, s28, 10\n"
        "s_mov_b32 s34, s39\n"
        "s_max_u32 s24, s28, s27\n"
        "s_cmp_ge_u32 s24, 2\n"
        "s_cselect_b64 vcc, -1, 0\n"
        "s_mov_b32 s35, 2\n"
        "buffer_load_dwordx4 v[8:11], v96, s[8:11], s32 offen \n"
        "buffer_load_dwordx4 v[12:15], v97, s[8:11], s32 offen \n"
        "buffer_load_dwordx4 v[16:19], v98, s[8:11], s32 offen \n"
        "buffer_load_dwordx4 v[20:23], v99, s[8:11], s32 offen \n"
        "buffer_load_dwordx4 v[24:27], v100, s[8:11], s32 offen \n"
        "buffer_load_dwordx4 v[28:31], v101, s[8:11], s32 offen \n"
        "buffer_load_dwordx4 v[32:35], v102, s[8:11], s32 offen \n"
        "buffer_load_dwordx4 v[36:39], v103, s[8:11], s32 offen \n"
        "s_lshr_b64 s[54:55], s[54:55], 4\n"
        "s_and_b32 s32, s54, 15\n"
        "s_mul_i32 s32, s32, 1179648\n"
        "s_waitcnt vmcnt(16)\n"
        "v_lshl_or_b32 v128, v132, 8, v128\n"
        "v_lshl_or_b32 v129, v133, 8, v129\n"
        "v_lshl_or_b32 v130, v134, 8, v130\n"
        "v_lshl_or_b32 v131, v135, 8, v131\n"
        "v_lshl_or_b32 v128, v136, 16, v128\n"
        "v_lshl_or_b32 v129, v137, 16, v129\n"
        "v_lshl_or_b32 v130, v138, 16, v130\n"
        "v_lshl_or_b32 v131, v139, 16, v131\n"
        "v_lshl_or_b32 v72, v140, 24, v128\n"
        "v_lshl_or_b32 v74, v141, 24, v129\n"
        "v_lshl_or_b32 v76, v142, 24, v130\n"
        "v_lshl_or_b32 v78, v143, 24, v131\n"
        "v_lshl_or_b32 v144, v148, 8, v144\n"
        "v_lshl_or_b32 v145, v149, 8, v145\n"
        "v_lshl_or_b32 v146, v150, 8, v146\n"
        "v_lshl_or_b32 v147, v151, 8, v147\n"
        "v_lshl_or_b32 v144, v152, 16, v144\n"
        "v_lshl_or_b32 v145, v153, 16, v145\n"
        "v_lshl_or_b32 v146, v154, 16, v146\n"
        "v_lshl_or_b32 v147, v155, 16, v147\n"
        "v_lshl_or_b32 v73, v156, 24, v144\n"
        "v_lshl_or_b32 v75, v157, 24, v145\n"
        "v_lshl_or_b32 v77, v158, 24, v146\n"
        "v_lshl_or_b32 v79, v159, 24, v147\n"
        "ds_write_b128 v120, v[72:75] offset:17408\n"
        "ds_write_b128 v120, v[76:79] offset:21632\n"
        "s_add_u32 m0, s34, 0\n"
        "s_nop 0\n"
        "buffer_load_dwordx4 v104, s[4:7], s33 offen lds\n"
        "s_add_u32 m0, s34, 4096\n"
        "s_nop 0\n"
        "buffer_load_dwordx4 v105, s[4:7], s33 offen lds\n"
        "s_waitcnt vmcnt(10)\n"
        "v_lshl_or_b32 v40, v44, 8, v40\n"
        "v_lshl_or_b32 v41, v45, 8, v41\n"
        "v_lshl_or_b32 v42, v46, 8, v42\n"
        "v_lshl_or_b32 v43, v47, 8, v43\n"
        "v_lshl_or_b32 v40, v48, 16, v40\n"
        "v_lshl_or_b32 v41, v49, 16, v41\n"
        "v_lshl_or_b32 v42, v50, 16, v42\n"
        "v_lshl_or_b32 v43, v51, 16, v43\n"
        "v_lshl_or_b32 v72, v52, 24, v40\n"
        "v_lshl_or_b32 v74, v53, 24, v41\n"
        "v_lshl_or_b32 v76, v54, 24, v42\n"
        "v_lshl_or_b32 v78, v55, 24, v43\n"
        "v_lshl_or_b32 v56, v60, 8, v56\n"
        "v_lshl_or_b32 v57, v61, 8, v57\n"
        "v_lshl_or_b32 v58, v62, 8, v58\n"
        "v_lshl_or_b32 v59, v63, 8, v59\n"
        "v_lshl_or_b32 v56, v64, 16, v56\n"
        "v_lshl_or_b32 v57, v65, 16, v57\n"
        "v_lshl_or_b32 v58, v66, 16, v58\n"
        "v_lshl_or_b32 v59, v67, 16, v59\n"
        "v_lshl_or_b32 v73, v68, 24, v56\n"
        "v_lshl_or_b32 v75, v69, 24, v57\n"
        "v_lshl_or_b32 v77, v70, 24, v58\n"
        "v_lshl_or_b32 v79, v71, 24, v59\n"
        "ds_write_b128 v120, v[72:75] offset:8704\n"
        "ds_write_b128 v120, v[76:79] offset:12928\n"
        "s_add_u32 m0, s34, 8192\n"
        "s_nop 0\n"
        "buffer_load_dwordx4 v106, s[4:7], s33 offen lds\n"
        "s_add_u32 m0, s34, 12288\n"
        "s_nop 0\n"
        "buffer_load_dwordx4 v107, s[4:7], s33 offen lds\n"
        "s_waitcnt lgkmcnt(0)\n"
        "s_barrier\n"
        "s_call_b64 s[56:57], .Lsq_xse\n"
        "s_branch .Lsq_h1x\n"
        ".Lsq_h1:\n"
        "s_call_b64 s[56:57], .Lsq_xs\n"
        ".Lsq_h1x:\n"
        "s_add_u32 m0, s34, 36864\n"
        "s_cbranch_vccnz .Lsq_xsb\n"
        "buffer_load_dwordx4 v113, s[4:7], s33 offen lds\n"
        ".Lsq_xsb:\n"
        "s_add_u32 s34, s34, 38912\n"
        "s_cmp_ge_u32 s34, 116736\n"
        "s_cselect_b32 s34, s39, s34\n"
        "s_lshr_b32 s52, s52, 4\n"
        "s_and_b32 s33, s52, 15\n"
        "s_lshl_b32 s33, s33, 7\n"
        "s_waitcnt vmcnt(9)\n"
        "buffer_load_dwordx4 v[40:43], v96, s[8:11], s32 offen \n"
        "v_lshl_or_b32 v8, v12, 8, v8\n"
        "v_lshl_or_b32 v9, v13, 8, v9\n"
        "v_lshl_or_b32 v10, v14, 8, v10\n"
        "buffer_load_dwordx4 v[44:47], v97, s[8:11], s32 offen \n"
        "v_lshl_or_b32 v11, v15, 8, v11\n"
        "v_lshl_or_b32 v8, v16, 16, v8\n"
        "v_lshl_or_b32 v9, v17, 16, v9\n"
        "buffer_load_dwordx4 v[48:51], v98, s[8:11], s32 offen \n"
        "v_lshl_or_b32 v10, v18, 16, v10\n"
        "v_lshl_or_b32 v11, v19, 16, v11\n"
        "v_lshl_or_b32 v72, v20, 24, v8\n"
        "buffer_load_dwordx4 v[52:55], v99, s[8:11], s32 offen \n"
        "v_lshl_or_b32 v74, v21, 24, v9\n"
        "v_lshl_or_b32 v76, v22, 24, v10\n"
        "v_lshl_or_b32 v78, v23, 24, v11\n"
        "buffer_load_dwordx4 v[56:59], v100, s[8:11], s32 offen \n"
        "v_lshl_or_b32 v24, v28, 8, v24\n"
        "v_lshl_or_b32 v25, v29, 8, v25\n"
        "v_lshl_or_b32 v26, v30, 8, v26\n"
        "buffer_load_dwordx4 v[60:63], v101, s[8:11], s32 offen \n"
        "v_lshl_or_b32 v27, v31, 8, v27\n"
        "v_lshl_or_b32 v24, v32, 16, v24\n"
        "v_lshl_or_b32 v25, v33, 16, v25\n"
        "buffer_load_dwordx4 v[64:67], v102, s[8:11], s32 offen \n"
        "v_lshl_or_b32 v26, v34, 16, v26\n"
        "v_lshl_or_b32 v27, v35, 16, v27\n"
        "v_lshl_or_b32 v73, v36, 24, v24\n"
        "buffer_load_dwordx4 v[68:71], v103, s[8:11], s32 offen \n"
        "v_lshl_or_b32 v75, v37, 24, v25\n"
        "v_lshl_or_b32 v77, v38, 24, v26\n"
        "v_lshl_or_b32 v79, v39, 24, v27\n"
        "ds_write_b128 v120, v[72:75] offset:0\n"
        "ds_write_b128 v120, v[76:79] offset:4224\n"
        ".Lsq_h1y:\n"
        "s_lshr_b64 s[54:55], s[54:55], 4\n"
        "s_and_b32 s32, s54, 15\n"
        "s_mul_i32 s32, s32, 1179648\n"
        "s_waitcnt vmcnt(8) lgkmcnt(0)\n"
        "s_barrier\n"
        "s_call_b64 s[56:57], .Lsq_xs\n"
        "s_add_u32 m0, s34, 36864\n"
        "s_cbranch_vccnz .Lsq_xsa\n"
        "buffer_load_dwordx4 v113, s[4:7], s33 offen lds\n"
        ".Lsq_xsa:\n"
        "s_add_u32 s34, s34, 38912\n"
        "s_cmp_ge_u32 s34, 116736\n"
        "s_cselect_b32 s34, s39, s34\n"
        "s_lshr_b32 s52, s52, 4\n"
        "s_and_b32 s33, s52, 15\n"
        "s_lshl_b32 s33, s33, 7\n"
        "s_waitcnt vmcnt(9)\n"
        "buffer_load_dwordx4 v[8:11], v96, s[8:11], s32 offen \n"
        "v_lshl_or_b32 v40, v44, 8, v40\n"
        "v_lshl_or_b32 v41, v45, 8, v41\n"
        "v_lshl_or_b32 v42, v46, 8, v42\n"
        "buffer_load_dwordx4 v[12:15], v97, s[8:11], s32 offen \n"
        "v_lshl_or_b32 v43, v47, 8, v43\n"
        "v_lshl_or_b32 v40, v48, 16, v40\n"
        "v_lshl_or_b32 v41, v49, 16, v41\n"
        "buffer_load_dwordx4 v[16:19], v98, s[8:11], s32 offen \n"
        "v_lshl_or_b32 v42, v50, 16, v42\n"
        "v_lshl_or_b32 v43, v51, 16, v43\n"
        "v_lshl_or_b32 v72, v52, 24, v40\n"
        "buffer_load_dwordx4 v[20:23], v99, s[8:11], s32 offen \n"
        "v_lshl_or_b32 v74, v53, 24, v41\n"
        "v_lshl_or_b32 v76, v54, 24, v42\n"
        "v_lshl_or_b32 v78, v55, 24, v43\n"
        "buffer_load_dwordx4 v[24:27], v100, s[8:11], s32 offen \n"
        "v_lshl_or_b32 v56, v60, 8, v56\n"
        "v_lshl_or_b32 v57, v61, 8, v57\n"
        "v_lshl_or_b32 v58, v62, 8, v58\n"
        "buffer_load_dwordx4 v[28:31], v101, s[8:11], s32 offen \n"
        "v_lshl_or_b32 v59, v63, 8, v59\n"
        "v_lshl_or_b32 v56, v64, 16, v56\n"
        "v_lshl_or_b32 v57, v65, 16, v57\n"
        "buffer_load_dwordx4 v[32:35], v102, s[8:11], s32 offen \n"
        "v_lshl_or_b32 v58, v66, 16, v58\n"
        "v_lshl_or_b32 v59, v67, 16, v59\n"
        "v_lshl_or_b32 v73, v68, 24, v56\n"
        "buffer_load_dwordx4 v[36:39], v103, s[8:11], s32 offen \n"
        "v_lshl_or_b32 v75, v69, 24, v57\n"
        "v_lshl_or_b32 v77, v70, 24, v58\n"
        "v_lshl_or_b32 v79, v71, 24, v59\n"
        "ds_write_b128 v120, v[72:75] offset:8704\n"
        "ds_write_b128 v120, v[76:79] offset:12928\n"
        "s_lshr_b64 s[54:55], s[54:55], 4\n"
        "s_and_b32 s32, s54, 15\n"
        "s_mul_i32 s32, s32, 1179648\n"
        "s_sub_u32 s35, s35, 1\n"
        "s_cbranch_scc1 .Lsq_hx\n"
        "s_waitcnt vmcnt(8) lgkmcnt(0)\n"
        "s_barrier\n"
        "s_branch .Lsq_h1\n"
        ".Lsq_hx:\n"
        "s_waitcnt vmcnt(8) lgkmcnt(0)\n"
        "s_barrier\n"
        "v_add_u32 v104, 0, v116\n"
        "v_add_u32 v105, 0, v117\n"
        "v_add_u32 v106, 0, v118\n"
        "v_add_u32 v107, 36864, v116\n"
        "v_add_u32 v108, 36864, v117\n"
        "v_add_u32 v109, 36864, v118\n"
        "v_add_u32 v110, 73728, v116\n"
        "v_add_u32 v111, 73728, v117\n"
        "v_add_u32 v112, 73728, v118\n"
        "s_mov_b32 s33, 0\n"
        "s_call_b64 s[56:57], .Lsq_xs\n"
        "s_add_u32 s34, s34, 36864\n"
        "s_add_u32 s33, s33, 110592\n"
        "s_mov_b32 s14, 18874368\n"
        "s_mov_b32 s15, 0x20000\n"
        "v_lshrrev_b32 v2, 4, v1\n"
        "v_lshl_add_u32 v2, s28, 2, v2\n"
        "v_and_b32 v3, 15, v1\n"
        "v_lshrrev_b32 v6, 2, v3\n"
        "v_mul_u32_u24 v6, 8448, v6\n"
        "v_lshl_add_u32 v6, v2, 6, v6\n"
        "v_lshrrev_b32 v7, 1, v2\n"
        "v_xor_b32 v7, v7, v3\n"
        "v_and_b32 v7, 3, v7\n"
        "v_lshl_add_u32 v4, v7, 4, v6\n"
        "v_add_u32 v4, 116736, v4\n"
        "v_mul_u32_u24 v5, 9216, v2\n"
        "v_lshl_add_u32 v5, v3, 4, v5\n"
        "s_lshl_b32 s24, s26, 8\n"
        "v_add_u32 v5, s24, v5\n"
        "s_mul_i32 s32, s29, 147456\n"
        "s_waitcnt vmcnt(9)\n"
        "v_lshl_or_b32 v8, v12, 8, v8\n"
        "v_lshl_or_b32 v9, v13, 8, v9\n"
        "v_lshl_or_b32 v10, v14, 8, v10\n"
        "v_lshl_or_b32 v11, v15, 8, v11\n"
        "v_lshl_or_b32 v8, v16, 16, v8\n"
        "v_lshl_or_b32 v9, v17, 16, v9\n"
        "v_lshl_or_b32 v10, v18, 16, v10\n"
        "v_lshl_or_b32 v11, v19, 16, v11\n"
        "v_lshl_or_b32 v72, v20, 24, v8\n"
        "v_lshl_or_b32 v74, v21, 24, v9\n"
        "v_lshl_or_b32 v76, v22, 24, v10\n"
        "v_lshl_or_b32 v78, v23, 24, v11\n"
        "v_lshl_or_b32 v24, v28, 8, v24\n"
        "v_lshl_or_b32 v25, v29, 8, v25\n"
        "v_lshl_or_b32 v26, v30, 8, v26\n"
        "v_lshl_or_b32 v27, v31, 8, v27\n"
        "v_lshl_or_b32 v24, v32, 16, v24\n"
        "v_lshl_or_b32 v25, v33, 16, v25\n"
        "v_lshl_or_b32 v26, v34, 16, v26\n"
        "v_lshl_or_b32 v27, v35, 16, v27\n"
        "v_lshl_or_b32 v73, v36, 24, v24\n"
        "v_lshl_or_b32 v75, v37, 24, v25\n"
        "v_lshl_or_b32 v77, v38, 24, v26\n"
        "v_lshl_or_b32 v79, v39, 24, v27\n"
        "ds_write_b128 v120, v[72:75] offset:0\n"
        "ds_write_b128 v120, v[76:79] offset:4224\n"
        "s_waitcnt vmcnt(0) lgkmcnt(0)\n"
        "s_barrier\n"
        "s_call_b64 s[56:57], .Lsq_xs\n"
        "s_add_u32 s34, s34, 36864\n"
        "s_add_u32 s33, s33, 110592\n"
        "s_barrier\n"
        "s_add_u32 m0, s34, 0\n"
        "s_nop 0\n"
        "buffer_load_dwordx4 v104, s[4:7], s33 offen lds\n"
        "s_add_u32 m0, s34, 4096\n"
        "s_nop 0\n"
        "buffer_load_dwordx4 v105, s[4:7], s33 offen lds\n"
        "s_barrier\n"
        "ds_read_b128 v[8:11], v4 offset:0\n"
        "s_waitcnt lgkmcnt(0)\n"
        "buffer_store_dwordx4 v[8:11], v5, s[12:15], s32 offen sc1\n"
        "s_add_u32 s32, s32, 147456\n"
        "s_add_u32 m0, s34, 8192\n"
        "s_nop 0\n"
        "buffer_load_dwordx4 v106, s[4:7], s33 offen lds\n"
        "s_add_u32 m0, s34, 12288\n"
        "s_nop 0\n"
        "buffer_load_dwordx4 v107, s[4:7], s33 offen lds\n"
        "s_waitcnt vmcnt(5)\n"
        "s_barrier\n"
        "ds_read_b128 v[12:15], v4 offset:1024\n"
        "s_waitcnt lgkmcnt(0)\n"
        "buffer_store_dwordx4 v[12:15], v5, s[12:15], s32 offen sc1\n"
        "s_add_u32 s32, s32, 147456\n"
        "s_add_u32 m0, s34, 16384\n"
        "s_nop 0\n"
        "buffer_load_dwordx4 v108, s[4:7], s33 offen lds\n"
        "s_add_u32 m0, s34, 20480\n"
        "s_nop 0\n"
        "buffer_load_dwordx4 v109, s[4:7], s33 offen lds\n"
        "s_barrier\n"
        "ds_read_b128 v[16:19], v4 offset:2048\n"
        "s_waitcnt lgkmcnt(0)\n"
        "buffer_store_dwordx4 v[16:19], v5, s[12:15], s32 offen sc1\n"
        "s_add_u32 s32, s32, 147456\n"
        "s_add_u32 m0, s34, 24576\n"
        "s_nop 0\n"
        "buffer_load_dwordx4 v110, s[4:7], s33 offen lds\n"
        "s_add_u32 m0, s34, 28672\n"
        "s_nop 0\n"
        "buffer_load_dwordx4 v111, s[4:7], s33 offen lds\n"
        "s_barrier\n"
        "ds_read_b128 v[20:23], v4 offset:3072\n"
        "s_waitcnt lgkmcnt(0)\n"
        "buffer_store_dwordx4 v[20:23], v5, s[12:15], s32 offen sc1\n"
        "s_add_u32 s32, s32, 147456\n"
        "s_add_u32 m0, s34, 32768\n"
        "s_nop 0\n"
        "buffer_load_dwordx4 v112, s[4:7], s33 offen lds\n"
        "s_add_u32 s33, s33, 110592\n"
        "s_cmp_ge_u32 s27, 2\n"
        "s_cbranch_scc1 .Lsq_nx\n"
        "s_add_u32 m0, s34, 36864\n"
        "s_nop 0\n"
        "buffer_load_dwordx4 v104, s[4:7], s33 offen lds\n"
        "s_cbranch_vccnz .Lsq_nx\n"
        "s_add_u32 m0, s34, 40960\n"
        "s_nop 0\n"
        "buffer_load_dwordx4 v105, s[4:7], s33 offen lds\n"
        ".Lsq_nx:\n"
        "s_barrier\n"
        "ds_read_b128 v[24:27], v4 offset:4096\n"
        "s_waitcnt lgkmcnt(0)\n"
        "buffer_store_dwordx4 v[24:27], v5, s[12:15], s32 offen sc1\n"
        "s_add_u32 s32, s32, 147456\n"
        "s_barrier\n"
        "ds_read_b128 v[28:31], v4 offset:5120\n"
        "s_waitcnt lgkmcnt(0)\n"
        "buffer_store_dwordx4 v[28:31], v5, s[12:15], s32 offen sc1\n"
        "s_add_u32 s32, s32, 147456\n"
        "s_barrier\n"
        "ds_read_b128 v[8:11], v4 offset:6144\n"
        "s_waitcnt lgkmcnt(0)\n"
        "buffer_store_dwordx4 v[8:11], v5, s[12:15], s32 offen sc1\n"
        "s_add_u32 s32, s32, 147456\n"
        "s_waitcnt vmcnt(14)\n"
        "s_barrier\n"
        "ds_read_b128 v[12:15], v4 offset:7168\n"
        "s_waitcnt lgkmcnt(0)\n"
        "buffer_store_dwordx4 v[12:15], v5, s[12:15], s32 offen sc1\n"
        "s_add_u32 s32, s32, 147456\n"
        "s_waitcnt vmcnt(12)\n"
        "s_barrier\n"
        "ds_read_b128 v[16:19], v4 offset:0\n"
        "s_waitcnt lgkmcnt(0)\n"
        "buffer_store_dwordx4 v[16:19], v5, s[12:15], s32 offen sc1\n"
        "s_add_u32 s32, s32, 147456\n"
        "s_waitcnt vmcnt(10)\n"
        "s_barrier\n"
        "ds_read_b128 v[20:23], v4 offset:1024\n"
        "s_waitcnt lgkmcnt(0)\n"
        "buffer_store_dwordx4 v[20:23], v5, s[12:15], s32 offen sc1\n"
        "s_add_u32 s32, s32, 147456\n"
        "s_barrier\n"
        "ds_read_b128 v[24:27], v4 offset:2048\n"
        "s_waitcnt lgkmcnt(0)\n"
        "buffer_store_dwordx4 v[24:27], v5, s[12:15], s32 offen sc1\n"
        "s_add_u32 s32, s32, 147456\n"
        "s_waitcnt vmcnt(9)\n"
        "s_barrier\n"
        "ds_read_b128 v[28:31], v4 offset:3072\n"
        "s_waitcnt lgkmcnt(0)\n"
        "buffer_store_dwordx4 v[28:31], v5, s[12:15], s32 offen sc1\n"
        "s_add_u32 s32, s32, 147456\n"
        "s_waitcnt vmcnt(8)\n"
        "s_barrier\n"
        "ds_read_b128 v[8:11], v4 offset:4096\n"
        "s_waitcnt lgkmcnt(0)\n"
        "buffer_store_dwordx4 v[8:11], v5, s[12:15], s32 offen sc1\n"
        "s_add_u32 s32, s32, 147456\n"
        "s_barrier\n"
        "ds_read_b128 v[12:15], v4 offset:5120\n"
        "s_waitcnt lgkmcnt(0)\n"
        "buffer_store_dwordx4 v[12:15], v5, s[12:15], s32 offen sc1\n"
        "s_add_u32 s32, s32, 147456\n"
        "s_barrier\n"
        "ds_read_b128 v[16:19], v4 offset:6144\n"
        "s_waitcnt lgkmcnt(0)\n"
        "buffer_store_dwordx4 v[16:19], v5, s[12:15], s32 offen sc1\n"
        "s_add_u32 s32, s32, 147456\n"
        "s_barrier\n"
        "ds_read_b128 v[20:23], v4 offset:7168\n"
        "s_waitcnt lgkmcnt(0)\n"
        "buffer_store_dwordx4 v[20:23], v5, s[12:15], s32 offen sc1\n"
        "s_add_u32 s32, s32, 147456\n"
        "s_cmp_ge_u32 s27, 2\n"
        "s_cbranch_scc1 .Lsq_l2\n"
        "s_barrier\n"
        "ds_read_b128 v[24:27], v4 offset:0\n"
        "s_waitcnt lgkmcnt(0)\n"
        "buffer_store_dwordx4 v[24:27], v5, s[12:15], s32 offen sc1\n"
        "s_add_u32 s32, s32, 147456\n"
        "v_add_u32 v4, 1024, v4\n"
        ".Lsq_l2:\n"
        "s_barrier\n"
        "ds_read_b128 v[40:43], v4 offset:0\n"
        "ds_read_b128 v[44:47], v4 offset:1024\n"
        "s_waitcnt lgkmcnt(1)\n"
        "buffer_store_dwordx4 v[40:43], v5, s[12:15], s32 offen sc1\n"
        "s_add_u32 s32, s32, 147456\n"
        "s_waitcnt lgkmcnt(0)\n"
        "buffer_store_dwordx4 v[44:47], v5, s[12:15], s32 offen sc1\n"
        "s_add_u32 s32, s32, 147456\n"
        ".Lsq_hend:\n"
        "s_endpgm\n"
        ".Lsq_xs:\n"
        "s_add_u32 m0, s34, 0\n"
        "s_nop 0\n"
        "buffer_load_dwordx4 v104, s[4:7], s33 offen lds\n"
        "s_add_u32 m0, s34, 4096\n"
        "s_nop 0\n"
        "buffer_load_dwordx4 v105, s[4:7], s33 offen lds\n"
        "s_add_u32 m0, s34, 8192\n"
        "s_nop 0\n"
        "buffer_load_dwordx4 v106, s[4:7], s33 offen lds\n"
        "s_add_u32 m0, s34, 12288\n"
        "s_nop 0\n"
        "buffer_load_dwordx4 v107, s[4:7], s33 offen lds\n"
        ".Lsq_xse:\n"
        "s_add_u32 m0, s34, 16384\n"
        "s_nop 0\n"
        "buffer_load_dwordx4 v108, s[4:7], s33 offen lds\n"
        "s_add_u32 m0, s34, 20480\n"
        "s_nop 0\n"
        "buffer_load_dwordx4 v109, s[4:7], s33 offen lds\n"
        "s_add_u32 m0, s34, 24576\n"
        "s_nop 0\n"
        "buffer_load_dwordx4 v110, s[4:7], s33 offen lds\n"
        "s_add_u32 m0, s34, 28672\n"
        "s_nop 0\n"
        "buffer_load_dwordx4 v111, s[4:7], s33 offen lds\n"
        "s_add_u32 m0, s34, 32768\n"
        "s_nop 0\n"
        "buffer_load_dwordx4 v112, s[4:7], s33 offen lds\n"
        "s_setpc_b64 s[56:57]\n"
        ".Lsq_compute:\n"
        "s_lshl_b32 s24, s28, 10\n"
        "s_add_u32 s24, s24, 150528\n"
        "v_lshl_add_u32 v45, v11, 6, s24\n"
        "v_lshl_add_u32 v44, v10, 2, v45\n"
        "v_and_b32 v12, 1, v1\n"
        "v_mul_u32_u24 v12, 4224, v12\n"
        "v_lshl_add_u32 v12, v10, 9, v12\n"
        "v_lshrrev_b32 v13, 1, v11\n"
        "v_lshl_add_u32 v12, v13, 4, v12\n"
        "s_lshl_b32 s24, s28, 7\n"
        "s_add_u32 s24, s24, 116736\n"
        "v_add_u32 v0, s24, v12\n"
        "v_xor_b32 v13, v13, v10\n"
        "v_lshlrev_b32 v2, 7, v11\n"
        "v_lshl_add_u32 v2, v13, 4, v2\n"
        "v_xor_b32 v3, 64, v2\n"
        "s_mul_i32 s24, s28, 8448\n"
        "s_add_u32 s24, s24, 116736\n"
        "v_and_b32 v13, 3, v13\n"
        "v_lshl_add_u32 v12, v11, 6, s24\n"
        "v_lshl_add_u32 v1, v13, 4, v12\n"
        "s_mov_b32 s40, 0x05010400\n"
        "s_mov_b32 s41, 0x07030602\n"
        "s_mov_b32 s42, 0x05040100\n"
        "s_mov_b32 s43, 0x07060302\n"
        "s_mov_b32 s44, 0x08080808\n"
        "s_mov_b32 s45, 0x0b0a0908\n"
        "s_cmp_ge_u32 s27, 2\n"
        "s_cselect_b64 vcc, -1, 0\n"
        "v_mov_b64 v[56:57], 0\n"
        "v_mov_b64 v[58:59], 0\n"
        "v_mov_b64 v[60:61], 0\n"
        "v_mov_b64 v[62:63], 0\n"
        "v_mov_b64 v[64:65], 0\n"
        "v_mov_b64 v[66:67], 0\n"
        "v_mov_b64 v[68:69], 0\n"
        "v_mov_b64 v[70:71], 0\n"
        "v_mov_b64 v[72:73], 0\n"
        "v_mov_b64 v[74:75], 0\n"
        "v_mov_b64 v[76:77], 0\n"
        "v_mov_b64 v[78:79], 0\n"
        "v_mov_b64 v[80:81], 0\n"
        "v_mov_b64 v[82:83], 0\n"
        "v_mov_b64 v[84:85], 0\n"
        "v_mov_b64 v[86:87], 0\n"
        "v_mov_b64 v[88:89], 0\n"
        "v_mov_b64 v[90:91], 0\n"
        "v_mov_b64 v[92:93], 0\n"
        "v_mov_b64 v[94:95], 0\n"
        "v_mov_b64 v[96:97], 0\n"
        "v_mov_b64 v[98:99], 0\n"
        "v_mov_b64 v[100:101], 0\n"
        "v_mov_b64 v[102:103], 0\n"
        "v_mov_b64 v[104:105], 0\n"
        "v_mov_b64 v[106:107], 0\n"
        "v_mov_b64 v[108:109], 0\n"
        "v_mov_b64 v[110:111], 0\n"
        "v_mov_b64 v[112:113], 0\n"
        "v_mov_b64 v[114:115], 0\n"
        "v_mov_b64 v[116:117], 0\n"
        "v_mov_b64 v[118:119], 0\n"
        "v_mov_b64 v[120:121], 0\n"
        "v_mov_b64 v[122:123], 0\n"
        "v_mov_b64 v[124:125], 0\n"
        "v_mov_b64 v[126:127], 0\n"
        "v_mov_b64 v[128:129], 0\n"
        "v_mov_b64 v[130:131], 0\n"
        "v_mov_b64 v[132:133], 0\n"
        "v_mov_b64 v[134:135], 0\n"
        "v_mov_b64 v[136:137], 0\n"
        "v_mov_b64 v[138:139], 0\n"
        "v_mov_b64 v[140:141], 0\n"
        "v_mov_b64 v[142:143], 0\n"
        "v_mov_b64 v[144:145], 0\n"
        "v_mov_b64 v[146:147], 0\n"
        "v_mov_b64 v[148:149], 0\n"
        "v_mov_b64 v[150:151], 0\n"
        "v_mov_b64 v[152:153], 0\n"
        "v_mov_b64 v[154:155], 0\n"
        "v_mov_b64 v[156:157], 0\n"
        "v_mov_b64 v[158:159], 0\n"
        "v_mov_b64 v[160:161], 0\n"
        "v_mov_b64 v[162:163], 0\n"
        "v_mov_b64 v[164:165], 0\n"
        "v_mov_b64 v[166:167], 0\n"
        "v_mov_b64 v[168:169], 0\n"
        "v_mov_b64 v[170:171], 0\n"
        "v_mov_b64 v[172:173], 0\n"
        "v_mov_b64 v[174:175], 0\n"
        "v_mov_b64 v[176:177], 0\n"
        "v_mov_b64 v[178:179], 0\n"
        "v_mov_b64 v[180:181], 0\n"
        "v_mov_b64 v[182:183], 0\n"
        "v_mov_b64 v[184:185], 0\n"
        "v_mov_b64 v[186:187], 0\n"
        "v_mov_b64 v[188:189], 0\n"
        "v_mov_b64 v[190:191], 0\n"
        "v_mov_b64 v[192:193], 0\n"
        "v_mov_b64 v[194:195], 0\n"
        "v_mov_b64 v[196:197], 0\n"
        "v_mov_b64 v[198:199], 0\n"
        "v_mov_b64 v[200:201], 0\n"
        "v_mov_b64 v[202:203], 0\n"
        "v_mov_b64 v[204:205], 0\n"
        "v_mov_b64 v[206:207], 0\n"
        "s_waitcnt vmcnt(0)\n"
        "v_perm_b32 v36, v33, v32, s40\n"
        "v_perm_b32 v37, v33, v32, s41\n"
        "v_perm_b32 v38, v35, v34, s40\n"
        "v_perm_b32 v39, v35, v34, s41\n"
        "v_perm_b32 v40, v38, v36, s42\n"
        "v_perm_b32 v41, v38, v36, s43\n"
        "v_perm_b32 v42, v39, v37, s42\n"
        "v_perm_b32 v43, v39, v37, s43\n"
        "ds_write2_b32 v44, v40, v41 offset0:0 offset1:4\n"
        "ds_write2_b32 v44, v42, v43 offset0:8 offset1:12\n"
        "s_waitcnt lgkmcnt(0)\n"
        "ds_read_b128 v[4:7], v45 offset:0\n"
        "ds_read_b128 v[8:11], v45 offset:16\n"
        "ds_read_b128 v[12:15], v45 offset:32\n"
        "ds_read_b128 v[16:19], v45 offset:48\n"
        "s_waitcnt lgkmcnt(0)\n"
        "v_xor_b32 v5, 0xffffff00, v5\n"
        "v_xor_b32 v7, 0xffffff00, v7\n"
        "v_mov_b32 v20, v4\n"
        "v_mov_b32 v21, v6\n"
        "v_xor_b32 v9, 0xffffff00, v9\n"
        "v_xor_b32 v11, 0xffffff00, v11\n"
        "v_mov_b32 v22, v8\n"
        "v_mov_b32 v23, v10\n"
        "v_xor_b32 v13, 0xffffff00, v13\n"
        "v_xor_b32 v15, 0xffffff00, v15\n"
        "v_mov_b32 v24, v12\n"
        "v_mov_b32 v25, v14\n"
        "v_xor_b32 v17, 0xffffff00, v17\n"
        "v_xor_b32 v19, 0xffffff00, v19\n"
        "v_mov_b32 v26, v16\n"
        "v_mov_b32 v27, v18\n"
        "s_mov_b32 s35, 3\n"
        ".Lsq_fix:\n"
        "v_perm_b32 v28, v5, v4, s45\n"
        "v_perm_b32 v29, v7, v6, s45\n"
        "v_perm_b32 v30, v9, v8, s45\n"
        "v_perm_b32 v31, v11, v10, s45\n"
        "v_perm_b32 v32, v13, v12, s45\n"
        "v_perm_b32 v33, v15, v14, s45\n"
        "v_perm_b32 v34, v17, v16, s45\n"
        "v_perm_b32 v35, v19, v18, s45\n"
        "s_sub_u32 s35, s35, 1\n"
        "s_cmp_lg_u32 s35, 0\n"
        "v_xor_b32 v4, v20, v29\n"
        "v_xor_b32 v6, v21, v28\n"
        "v_xor_b32 v8, v22, v31\n"
        "v_xor_b32 v10, v23, v30\n"
        "v_xor_b32 v12, v24, v33\n"
        "v_xor_b32 v14, v25, v32\n"
        "v_xor_b32 v16, v26, v35\n"
        "v_xor_b32 v18, v27, v34\n"
        "s_cbranch_scc1 .Lsq_fix\n"
        "v_add_u32 v0, 17408, v0\n"
        "s_barrier\n"
        "s_mov_b32 s33, -8704\n"
        "s_call_b64 s[56:57], .Lsq_dq0\n"
        "s_call_b64 s[56:57], .Lsq_dq1\n"
        "v_mov_b64 v[224:225], v[208:209]\n"
        "v_mov_b64 v[226:227], v[210:211]\n"
        "v_mov_b64 v[228:229], v[212:213]\n"
        "v_mov_b64 v[230:231], v[214:215]\n"
        "v_mov_b64 v[232:233], v[216:217]\n"
        "v_mov_b64 v[234:235], v[218:219]\n"
        "v_mov_b64 v[236:237], v[220:221]\n"
        "v_mov_b64 v[238:239], v[222:223]\n"
        "s_mov_b32 s33, -8704\n"
        "s_call_b64 s[56:57], .Lsq_dq0\n"
        "s_call_b64 s[56:57], .Lsq_dq1\n"
        "v_mov_b64 v[240:241], v[208:209]\n"
        "v_mov_b64 v[242:243], v[210:211]\n"
        "v_mov_b64 v[244:245], v[212:213]\n"
        "v_mov_b64 v[246:247], v[214:215]\n"
        "v_mov_b64 v[248:249], v[216:217]\n"
        "v_mov_b64 v[250:251], v[218:219]\n"
        "v_mov_b64 v[252:253], v[220:221]\n"
        "v_mov_b64 v[254:255], v[222:223]\n"
        "s_mov_b32 s35, 0\n"
        "s_mov_b32 s36, -77824\n"
        "s_mov_b32 s37, 38912\n"
        "s_mov_b32 s38, 6\n"
        "s_barrier\n"
        "ds_read_b128 v[32:35], v2 offset:0\n"
        "ds_read_b128 v[36:39], v2 offset:2048\n"
        "ds_read_b128 v[40:43], v2 offset:4096\n"
        "ds_read_b128 v[44:47], v2 offset:6144\n"
        "s_call_b64 s[56:57], .Lsq_dq0\n"
        "s_setprio 1\n"
        ".Lsq_kb:\n"
        "s_waitcnt lgkmcnt(3)\n"
        "v_mfma_f32_16x16x32_bf16 v[56:59], v[208:211], v[32:35], v[56:59]\n"
        "ds_read_b128 v[48:51], v2 offset:8192\n"
        "v_perm_b32 v28, v5, v4, v24\n"
        "v_mfma_f32_16x16x32_bf16 v[60:63], v[212:215], v[32:35], v[60:63]\n"
        "v_perm_b32 v30, v9, v8, v24\n"
        "s_waitcnt lgkmcnt(3)\n"
        "v_mfma_f32_16x16x32_bf16 v[64:67], v[208:211], v[36:39], v[64:67]\n"
        "ds_read_b128 v[32:35], v2 offset:10240\n"
        "v_xor_b32_e64 v24, s44, v24\n"
        "v_mfma_f32_16x16x32_bf16 v[68:71], v[212:215], v[36:39], v[68:71]\n"
        "v_perm_b32 v29, v7, v6, v24\n"
        "s_waitcnt lgkmcnt(3)\n"
        "v_mfma_f32_16x16x32_bf16 v[72:75], v[208:211], v[40:43], v[72:75]\n"
        "ds_read_b128 v[36:39], v2 offset:12288\n"
        "v_perm_b32 v31, v11, v10, v24\n"
        "v_mfma_f32_16x16x32_bf16 v[76:79], v[212:215], v[40:43], v[76:79]\n"
        "v_xor_b32 v28, v28, v29\n"
        "s_waitcnt lgkmcnt(3)\n"
        "v_mfma_f32_16x16x32_bf16 v[80:83], v[208:211], v[44:47], v[80:83]\n"
        "ds_read_b128 v[40:43], v2 offset:14336\n"
        "v_xor_b32 v30, v30, v31\n"
        "v_mfma_f32_16x16x32_bf16 v[84:87], v[212:215], v[44:47], v[84:87]\n"
        "v_perm_b32 v216, v30, v28, s40\n"
        "s_waitcnt lgkmcnt(3)\n"
        "v_mfma_f32_16x16x32_bf16 v[88:91], v[208:211], v[48:51], v[88:91]\n"
        "ds_read_b128 v[44:47], v2 offset:16384\n"
        "v_perm_b32 v217, v30, v28, s41\n"
        "v_mfma_f32_16x16x32_bf16 v[92:95], v[212:215], v[48:51], v[92:95]\n"
        "v_perm_b32 v28, v5, v4, v25\n"
        "s_waitcnt lgkmcnt(3)\n"
        "v_mfma_f32_16x16x32_bf16 v[96:99], v[208:211], v[32:35], v[96:99]\n"
        "ds_read_b128 v[48:51], v2 offset:18432\n"
        "v_perm_b32 v30, v9, v8, v25\n"
        "v_mfma_f32_16x16x32_bf16 v[100:103], v[212:215], v[32:35], v[100:103]\n"
        "v_xor_b32_e64 v25, s44, v25\n"
        "s_waitcnt lgkmcnt(3)\n"
        "v_mfma_f32_16x16x32_bf16 v[104:107], v[208:211], v[36:39], v[104:107]\n"
        "ds_read_b128 v[32:35], v2 offset:20480\n"
        "v_perm_b32 v29, v7, v6, v25\n"
        "v_mfma_f32_16x16x32_bf16 v[108:111], v[212:215], v[36:39], v[108:111]\n"
        "v_perm_b32 v31, v11, v10, v25\n"
        "s_waitcnt lgkmcnt(3)\n"
        "v_mfma_f32_16x16x32_bf16 v[112:115], v[208:211], v[40:43], v[112:115]\n"
        "ds_read_b128 v[36:39], v2 offset:22528\n"
        "v_xor_b32 v28, v28, v29\n"
        "v_mfma_f32_16x16x32_bf16 v[116:119], v[212:215], v[40:43], v[116:119]\n"
        "v_xor_b32 v30, v30, v31\n"
        "s_waitcnt lgkmcnt(3)\n"
        "v_mfma_f32_16x16x32_bf16 v[120:123], v[208:211], v[44:47], v[120:123]\n"
        "ds_read_b128 v[40:43], v2 offset:24576\n"
        "v_perm_b32 v218, v30, v28, s40\n"
        "v_mfma_f32_16x16x32_bf16 v[124:127], v[212:215], v[44:47], v[124:127]\n"
        "v_perm_b32 v219, v30, v28, s41\n"
        "s_waitcnt lgkmcnt(3)\n"
        "v_mfma_f32_16x16x32_bf16 v[128:131], v[208:211], v[48:51], v[128:131]\n"
        "ds_read_b128 v[44:47], v2 offset:26624\n"
        "v_perm_b32 v28, v13, v12, v26\n"
        "v_mfma_f32_16x16x32_bf16 v[132:135], v[212:215], v[48:51], v[132:135]\n"
        "v_perm_b32 v30, v17, v16, v26\n"
        "s_waitcnt lgkmcnt(3)\n"
        "v_mfma_f32_16x16x32_bf16 v[136:139], v[208:211], v[32:35], v[136:139]\n"
        "ds_read_b128 v[48:51], v2 offset:28672\n"
        "v_xor_b32_e64 v26, s44, v26\n"
        "v_mfma_f32_16x16x32_bf16 v[140:143], v[212:215], v[32:35], v[140:143]\n"
        "v_perm_b32 v29, v15, v14, v26\n"
        "s_waitcnt lgkmcnt(3)\n"
        "v_mfma_f32_16x16x32_bf16 v[144:147], v[208:211], v[36:39], v[144:147]\n"
        "ds_read_b128 v[52:55], v2 offset:30720\n"
        "v_perm_b32 v31, v19, v18, v26\n"
        "v_mfma_f32_16x16x32_bf16 v[148:151], v[212:215], v[36:39], v[148:151]\n"
        "v_xor_b32 v28, v28, v29\n"
        "s_waitcnt lgkmcnt(3)\n"
        "v_mfma_f32_16x16x32_bf16 v[152:155], v[208:211], v[40:43], v[152:155]\n"
        "ds_read_b128 v[32:35], v2 offset:32768\n"
        "v_xor_b32 v30, v30, v31\n"
        "v_mfma_f32_16x16x32_bf16 v[156:159], v[212:215], v[40:43], v[156:159]\n"
        "v_perm_b32 v220, v30, v28, s40\n"
        "s_waitcnt lgkmcnt(3)\n"
        "v_mfma_f32_16x16x32_bf16 v[160:163], v[208:211], v[44:47], v[160:163]\n"
        "ds_read_b128 v[36:39], v2 offset:34816\n"
        "v_perm_b32 v221, v30, v28, s41\n"
        "v_mfma_f32_16x16x32_bf16 v[164:167], v[212:215], v[44:47], v[164:167]\n"
        "v_perm_b32 v28, v13, v12, v27\n"
        "s_waitcnt lgkmcnt(3)\n"
        "v_mfma_f32_16x16x32_bf16 v[168:171], v[208:211], v[48:51], v[168:171]\n"
        "ds_read_b128 v[40:43], v2 offset:36864\n"
        "v_perm_b32 v30, v17, v16, v27\n"
        "v_mfma_f32_16x16x32_bf16 v[172:175], v[212:215], v[48:51], v[172:175]\n"
        "v_xor_b32_e64 v27, s44, v27\n"
        "s_waitcnt lgkmcnt(3)\n"
        "v_mfma_f32_16x16x32_bf16 v[176:179], v[208:211], v[52:55], v[176:179]\n"
        "ds_read_b128 v[44:47], v3 offset:0\n"
        "v_perm_b32 v29, v15, v14, v27\n"
        "v_mfma_f32_16x16x32_bf16 v[180:183], v[212:215], v[52:55], v[180:183]\n"
        "v_perm_b32 v31, v19, v18, v27\n"
        "s_waitcnt lgkmcnt(3)\n"
        "v_mfma_f32_16x16x32_bf16 v[184:187], v[208:211], v[32:35], v[184:187]\n"
        "ds_read_b128 v[48:51], v3 offset:2048\n"
        "v_xor_b32 v28, v28, v29\n"
        "v_mfma_f32_16x16x32_bf16 v[188:191], v[212:215], v[32:35], v[188:191]\n"
        "v_xor_b32 v30, v30, v31\n"
        "s_waitcnt lgkmcnt(3)\n"
        "v_mfma_f32_16x16x32_bf16 v[192:195], v[208:211], v[36:39], v[192:195]\n"
        "ds_read_b128 v[52:55], v3 offset:4096\n"
        "v_perm_b32 v222, v30, v28, s40\n"
        "v_mfma_f32_16x16x32_bf16 v[196:199], v[212:215], v[36:39], v[196:199]\n"
        "v_perm_b32 v223, v30, v28, s41\n"
        "s_waitcnt lgkmcnt(3)\n"
        "ds_read_b128 v[32:35], v3 offset:6144\n"
        "s_cbranch_vccnz .Lsq_sk0\n"
        "v_mfma_f32_16x16x32_bf16 v[200:203], v[208:211], v[40:43], v[200:203]\n"
        "v_mfma_f32_16x16x32_bf16 v[204:207], v[212:215], v[40:43], v[204:207]\n"
        ".Lsq_sk0:\n"
        "s_waitcnt lgkmcnt(3)\n"
        "v_mfma_f32_16x16x32_bf16 v[56:59], v[216:219], v[44:47], v[56:59]\n"
        "ds_read_b128 v[36:39], v3 offset:8192\n"
        "s_add_u32 s35, s35, 1\n"
        "v_mfma_f32_16x16x32_bf16 v[60:63], v[220:223], v[44:47], v[60:63]\n"
        "s_cmp_eq_u32 s35, 3\n"
        "s_waitcnt lgkmcnt(3)\n"
        "v_mfma_f32_16x16x32_bf16 v[64:67], v[216:219], v[48:51], v[64:67]\n"
        "ds_read_b128 v[40:43], v3 offset:10240\n"
        "s_cselect_b32 s34, s36, s37\n"
        "v_mfma_f32_16x16x32_bf16 v[68:71], v[220:223], v[48:51], v[68:71]\n"
        "s_cselect_b32 s35, 0, s35\n"
        "s_waitcnt lgkmcnt(3)\n"
        "v_mfma_f32_16x16x32_bf16 v[72:75], v[216:219], v[52:55], v[72:75]\n"
        "ds_read_b128 v[44:47], v3 offset:12288\n"
        "v_mfma_f32_16x16x32_bf16 v[76:79], v[220:223], v[52:55], v[76:79]\n"
        "v_add_u32 v2, s34, v2\n"
        "s_waitcnt lgkmcnt(3)\n"
        "v_mfma_f32_16x16x32_bf16 v[80:83], v[216:219], v[32:35], v[80:83]\n"
        "ds_read_b128 v[48:51], v3 offset:14336\n"
        "v_mfma_f32_16x16x32_bf16 v[84:87], v[220:223], v[32:35], v[84:87]\n"
        "s_waitcnt lgkmcnt(3)\n"
        "v_mfma_f32_16x16x32_bf16 v[88:91], v[216:219], v[36:39], v[88:91]\n"
        "ds_read_b128 v[52:55], v3 offset:16384\n"
        "v_mfma_f32_16x16x32_bf16 v[92:95], v[220:223], v[36:39], v[92:95]\n"
        "s_waitcnt lgkmcnt(3)\n"
        "v_mfma_f32_16x16x32_bf16 v[96:99], v[216:219], v[40:43], v[96:99]\n"
        "s_barrier\n"
        "ds_read_b128 v[20:23], v0 offset:0\n"
        "ds_read_b128 v[24:27], v0 offset:2048\n"
        "ds_read_b128 v[32:35], v3 offset:18432\n"
        "v_mfma_f32_16x16x32_bf16 v[100:103], v[220:223], v[40:43], v[100:103]\n"
        "s_waitcnt lgkmcnt(5)\n"
        "v_mfma_f32_16x16x32_bf16 v[104:107], v[216:219], v[44:47], v[104:107]\n"
        "ds_read_b128 v[36:39], v3 offset:20480\n"
        "v_mfma_f32_16x16x32_bf16 v[108:111], v[220:223], v[44:47], v[108:111]\n"
        "s_waitcnt lgkmcnt(5)\n"
        "v_mfma_f32_16x16x32_bf16 v[112:115], v[216:219], v[48:51], v[112:115]\n"
        "ds_read_b128 v[40:43], v3 offset:22528\n"
        "v_mfma_f32_16x16x32_bf16 v[116:119], v[220:223], v[48:51], v[116:119]\n"
        "s_waitcnt lgkmcnt(3)\n"
        "v_mfma_f32_16x16x32_bf16 v[120:123], v[216:219], v[52:55], v[120:123]\n"
        "ds_read_b128 v[44:47], v3 offset:24576\n"
        "v_add_u32 v0, s33, v0\n"
        "v_perm_b32 v28, v5, v4, v20\n"
        "v_mfma_f32_16x16x32_bf16 v[124:127], v[220:223], v[52:55], v[124:127]\n"
        "v_perm_b32 v30, v9, v8, v20\n"
        "v_xor_b32_e64 v20, s44, v20\n"
        "v_perm_b32 v29, v7, v6, v20\n"
        "s_waitcnt lgkmcnt(3)\n"
        "v_mfma_f32_16x16x32_bf16 v[128:131], v[216:219], v[32:35], v[128:131]\n"
        "ds_read_b128 v[48:51], v3 offset:26624\n"
        "v_perm_b32 v31, v11, v10, v20\n"
        "v_xor_b32 v28, v28, v29\n"
        "v_mfma_f32_16x16x32_bf16 v[132:135], v[220:223], v[32:35], v[132:135]\n"
        "v_xor_b32 v30, v30, v31\n"
        "v_perm_b32 v208, v30, v28, s40\n"
        "s_sub_u32 s33, 0, s33\n"
        "s_waitcnt lgkmcnt(3)\n"
        "v_mfma_f32_16x16x32_bf16 v[136:139], v[216:219], v[36:39], v[136:139]\n"
        "ds_read_b128 v[32:35], v3 offset:28672\n"
        "v_perm_b32 v209, v30, v28, s41\n"
        "v_mfma_f32_16x16x32_bf16 v[140:143], v[220:223], v[36:39], v[140:143]\n"
        "v_perm_b32 v28, v5, v4, v21\n"
        "v_perm_b32 v30, v9, v8, v21\n"
        "s_waitcnt lgkmcnt(3)\n"
        "v_mfma_f32_16x16x32_bf16 v[144:147], v[216:219], v[40:43], v[144:147]\n"
        "ds_read_b128 v[36:39], v3 offset:30720\n"
        "v_xor_b32_e64 v21, s44, v21\n"
        "v_perm_b32 v29, v7, v6, v21\n"
        "v_mfma_f32_16x16x32_bf16 v[148:151], v[220:223], v[40:43], v[148:151]\n"
        "v_perm_b32 v31, v11, v10, v21\n"
        "v_xor_b32 v28, v28, v29\n"
        "s_waitcnt lgkmcnt(3)\n"
        "v_mfma_f32_16x16x32_bf16 v[152:155], v[216:219], v[44:47], v[152:155]\n"
        "ds_read_b128 v[40:43], v3 offset:32768\n"
        "v_xor_b32 v30, v30, v31\n"
        "v_mfma_f32_16x16x32_bf16 v[156:159], v[220:223], v[44:47], v[156:159]\n"
        "v_perm_b32 v210, v30, v28, s40\n"
        "v_perm_b32 v211, v30, v28, s41\n"
        "s_waitcnt lgkmcnt(3)\n"
        "v_mfma_f32_16x16x32_bf16 v[160:163], v[216:219], v[48:51], v[160:163]\n"
        "ds_read_b128 v[44:47], v3 offset:34816\n"
        "v_perm_b32 v28, v13, v12, v22\n"
        "v_perm_b32 v30, v17, v16, v22\n"
        "v_mfma_f32_16x16x32_bf16 v[164:167], v[220:223], v[48:51], v[164:167]\n"
        "v_xor_b32_e64 v22, s44, v22\n"
        "v_perm_b32 v29, v15, v14, v22\n"
        "s_waitcnt lgkmcnt(3)\n"
        "v_mfma_f32_16x16x32_bf16 v[168:171], v[216:219], v[32:35], v[168:171]\n"
        "ds_read_b128 v[48:51], v3 offset:36864\n"
        "v_perm_b32 v31, v19, v18, v22\n"
        "v_xor_b32 v28, v28, v29\n"
        "v_mfma_f32_16x16x32_bf16 v[172:175], v[220:223], v[32:35], v[172:175]\n"
        "v_xor_b32 v30, v30, v31\n"
        "v_perm_b32 v212, v30, v28, s40\n"
        "s_waitcnt lgkmcnt(3)\n"
        "v_mfma_f32_16x16x32_bf16 v[176:179], v[216:219], v[36:39], v[176:179]\n"
        "ds_read_b128 v[32:35], v2 offset:0\n"
        "v_perm_b32 v213, v30, v28, s41\n"
        "v_perm_b32 v28, v13, v12, v23\n"
        "v_mfma_f32_16x16x32_bf16 v[180:183], v[220:223], v[36:39], v[180:183]\n"
        "v_perm_b32 v30, v17, v16, v23\n"
        "v_add_u32 v3, s34, v3\n"
        "s_waitcnt lgkmcnt(3)\n"
        "v_mfma_f32_16x16x32_bf16 v[184:187], v[216:219], v[40:43], v[184:187]\n"
        "ds_read_b128 v[36:39], v2 offset:2048\n"
        "v_xor_b32_e64 v23, s44, v23\n"
        "v_perm_b32 v29, v15, v14, v23\n"
        "v_mfma_f32_16x16x32_bf16 v[188:191], v[220:223], v[40:43], v[188:191]\n"
        "v_perm_b32 v31, v19, v18, v23\n"
        "v_xor_b32 v28, v28, v29\n"
        "s_waitcnt lgkmcnt(3)\n"
        "v_mfma_f32_16x16x32_bf16 v[192:195], v[216:219], v[44:47], v[192:195]\n"
        "ds_read_b128 v[40:43], v2 offset:4096\n"
        "v_xor_b32 v30, v30, v31\n"
        "v_perm_b32 v214, v30, v28, s40\n"
        "v_mfma_f32_16x16x32_bf16 v[196:199], v[220:223], v[44:47], v[196:199]\n"
        "v_perm_b32 v215, v30, v28, s41\n"
        "s_sub_u32 s38, s38, 1\n"
        "s_cmp_lg_u32 s38, 0\n"
        "s_waitcnt lgkmcnt(3)\n"
        "ds_read_b128 v[44:47], v2 offset:6144\n"
        "s_cbranch_vccnz .Lsq_sk1\n"
        "v_mfma_f32_16x16x32_bf16 v[200:203], v[216:219], v[48:51], v[200:203]\n"
        "v_mfma_f32_16x16x32_bf16 v[204:207], v[220:223], v[48:51], v[204:207]\n"
        ".Lsq_sk1:\n"
        "s_cbranch_scc1 .Lsq_kb\n"
        "ds_read_b128 v[44:47], v3 offset:0\n"
        "ds_read_b128 v[48:51], v3 offset:2048\n"
        "ds_read_b128 v[52:55], v3 offset:4096\n"
        "s_waitcnt lgkmcnt(6)\n"
        "v_mfma_f32_16x16x32_bf16 v[56:59], v[224:227], v[32:35], v[56:59]\n"
        "v_perm_b32 v28, v5, v4, v24\n"
        "v_perm_b32 v30, v9, v8, v24\n"
        "v_xor_b32_e64 v24, s44, v24\n"
        "v_perm_b32 v29, v7, v6, v24\n"
        "v_mfma_f32_16x16x32_bf16 v[60:63], v[228:231], v[32:35], v[60:63]\n"
        "v_perm_b32 v31, v11, v10, v24\n"
        "v_xor_b32 v28, v28, v29\n"
        "v_xor_b32 v30, v30, v31\n"
        "v_perm_b32 v216, v30, v28, s40\n"
        "v_perm_b32 v217, v30, v28, s41\n"
        "s_waitcnt lgkmcnt(5)\n"
        "v_mfma_f32_16x16x32_bf16 v[56:59], v[240:243], v[36:39], v[56:59]\n"
        "v_perm_b32 v28, v5, v4, v25\n"
        "v_perm_b32 v30, v9, v8, v25\n"
        "v_xor_b32_e64 v25, s44, v25\n"
        "v_perm_b32 v29, v7, v6, v25\n"
        "v_mfma_f32_16x16x32_bf16 v[60:63], v[244:247], v[36:39], v[60:63]\n"
        "v_perm_b32 v31, v11, v10, v25\n"
        "v_xor_b32 v28, v28, v29\n"
        "v_xor_b32 v30, v30, v31\n"
        "v_perm_b32 v218, v30, v28, s40\n"
        "v_perm_b32 v219, v30, v28, s41\n"
        "s_waitcnt lgkmcnt(4)\n"
        "v_mfma_f32_16x16x32_bf16 v[56:59], v[208:211], v[40:43], v[56:59]\n"
        "v_perm_b32 v28, v13, v12, v26\n"
        "v_perm_b32 v30, v17, v16, v26\n"
        "v_xor_b32_e64 v26, s44, v26\n"
        "v_perm_b32 v29, v15, v14, v26\n"
        "v_mfma_f32_16x16x32_bf16 v[60:63], v[212:215], v[40:43], v[60:63]\n"
        "v_perm_b32 v31, v19, v18, v26\n"
        "v_xor_b32 v28, v28, v29\n"
        "v_xor_b32 v30, v30, v31\n"
        "v_perm_b32 v220, v30, v28, s40\n"
        "v_perm_b32 v221, v30, v28, s41\n"
        "s_waitcnt lgkmcnt(2)\n"
        "v_mfma_f32_16x16x32_bf16 v[56:59], v[232:235], v[44:47], v[56:59]\n"
        "v_perm_b32 v28, v13, v12, v27\n"
        "v_perm_b32 v30, v17, v16, v27\n"
        "v_xor_b32_e64 v27, s44, v27\n"
        "v_perm_b32 v29, v15, v14, v27\n"
        "v_mfma_f32_16x16x32_bf16 v[60:63], v[236:239], v[44:47], v[60:63]\n"
        "v_perm_b32 v31, v19, v18, v27\n"
        "v_xor_b32 v28, v28, v29\n"
        "v_xor_b32 v30, v30, v31\n"
        "v_perm_b32 v222, v30, v28, s40\n"
        "v_perm_b32 v223, v30, v28, s41\n"
        "s_waitcnt lgkmcnt(1)\n"
        "v_mfma_f32_16x16x32_bf16 v[56:59], v[248:251], v[48:51], v[56:59]\n"
        "ds_read_b128 v[4:7], v2 offset:6144\n"
        "ds_read_b128 v[8:11], v2 offset:8192\n"
        "ds_read_b128 v[12:15], v2 offset:10240\n"
        "v_mfma_f32_16x16x32_bf16 v[60:63], v[252:255], v[48:51], v[60:63]\n"
        "s_waitcnt lgkmcnt(3)\n"
        "v_mfma_f32_16x16x32_bf16 v[56:59], v[216:219], v[52:55], v[56:59]\n"
        "s_barrier\n"
        "ds_read_b128 v[16:19], v3 offset:6144\n"
        "ds_read_b128 v[20:23], v3 offset:8192\n"
        "ds_read_b128 v[24:27], v3 offset:10240\n"
        "v_mfma_f32_16x16x32_bf16 v[60:63], v[220:223], v[52:55], v[60:63]\n"
        "s_waitcnt lgkmcnt(5)\n"
        "v_mfma_f32_16x16x32_bf16 v[64:67], v[224:227], v[4:7], v[64:67]\n"
        "ds_read_b128 v[32:35], v2 offset:12288\n"
        "ds_read_b128 v[36:39], v2 offset:14336\n"
        "v_mfma_f32_16x16x32_bf16 v[68:71], v[228:231], v[4:7], v[68:71]\n"
        "s_waitcnt lgkmcnt(6)\n"
        "v_mfma_f32_16x16x32_bf16 v[64:67], v[240:243], v[8:11], v[64:67]\n"
        "ds_read_b128 v[40:43], v2 offset:16384\n"
        "ds_read_b128 v[44:47], v3 offset:12288\n"
        "v_mfma_f32_16x16x32_bf16 v[68:71], v[244:247], v[8:11], v[68:71]\n"
        "s_waitcnt lgkmcnt(7)\n"
        "v_mfma_f32_16x16x32_bf16 v[64:67], v[208:211], v[12:15], v[64:67]\n"
        "ds_read_b128 v[48:51], v3 offset:14336\n"
        "ds_read_b128 v[52:55], v3 offset:16384\n"
        "v_cvt_pk_bf16_f32 v56, v56, v60\n"
        "v_mfma_f32_16x16x32_bf16 v[68:71], v[212:215], v[12:15], v[68:71]\n"
        "v_cvt_pk_bf16_f32 v57, v57, v61\n"
        "s_waitcnt lgkmcnt(8)\n"
        "v_mfma_f32_16x16x32_bf16 v[64:67], v[232:235], v[16:19], v[64:67]\n"
        "ds_read_b128 v[4:7], v2 offset:18432\n"
        "v_cvt_pk_bf16_f32 v58, v58, v62\n"
        "v_mfma_f32_16x16x32_bf16 v[68:71], v[236:239], v[16:19], v[68:71]\n"
        "v_cvt_pk_bf16_f32 v59, v59, v63\n"
        "s_waitcnt lgkmcnt(8)\n"
        "v_mfma_f32_16x16x32_bf16 v[64:67], v[248:251], v[20:23], v[64:67]\n"
        "ds_read_b128 v[8:11], v2 offset:20480\n"
        "v_mfma_f32_16x16x32_bf16 v[68:71], v[252:255], v[20:23], v[68:71]\n"
        "ds_write_b128 v1, v[56:59] offset:0\n"
        "s_waitcnt lgkmcnt(9)\n"
        "v_mfma_f32_16x16x32_bf16 v[64:67], v[216:219], v[24:27], v[64:67]\n"
        "ds_read_b128 v[12:15], v2 offset:22528\n"
        "v_mfma_f32_16x16x32_bf16 v[68:71], v[220:223], v[24:27], v[68:71]\n"
        "s_waitcnt lgkmcnt(8)\n"
        "v_mfma_f32_16x16x32_bf16 v[72:75], v[224:227], v[32:35], v[72:75]\n"
        "ds_read_b128 v[16:19], v3 offset:18432\n"
        "v_mfma_f32_16x16x32_bf16 v[76:79], v[228:231], v[32:35], v[76:79]\n"
        "s_waitcnt lgkmcnt(2)\n"
        "v_mfma_f32_16x16x32_bf16 v[72:75], v[240:243], v[36:39], v[72:75]\n"
        "s_barrier\n"
        "ds_read_b128 v[20:23], v3 offset:20480\n"
        "v_mfma_f32_16x16x32_bf16 v[76:79], v[244:247], v[36:39], v[76:79]\n"
        "v_mfma_f32_16x16x32_bf16 v[72:75], v[208:211], v[40:43], v[72:75]\n"
        "ds_read_b128 v[24:27], v3 offset:22528\n"
        "v_cvt_pk_bf16_f32 v64, v64, v68\n"
        "v_mfma_f32_16x16x32_bf16 v[76:79], v[212:215], v[40:43], v[76:79]\n"
        "v_cvt_pk_bf16_f32 v65, v65, v69\n"
        "v_mfma_f32_16x16x32_bf16 v[72:75], v[232:235], v[44:47], v[72:75]\n"
        "ds_read_b128 v[32:35], v2 offset:24576\n"
        "v_cvt_pk_bf16_f32 v66, v66, v70\n"
        "v_mfma_f32_16x16x32_bf16 v[76:79], v[236:239], v[44:47], v[76:79]\n"
        "v_cvt_pk_bf16_f32 v67, v67, v71\n"
        "v_mfma_f32_16x16x32_bf16 v[72:75], v[248:251], v[48:51], v[72:75]\n"
        "ds_read_b128 v[36:39], v2 offset:26624\n"
        "v_mfma_f32_16x16x32_bf16 v[76:79], v[252:255], v[48:51], v[76:79]\n"
        "ds_write_b128 v1, v[64:67] offset:1024\n"
        "v_mfma_f32_16x16x32_bf16 v[72:75], v[216:219], v[52:55], v[72:75]\n"
        "ds_read_b128 v[40:43], v2 offset:28672\n"
        "v_mfma_f32_16x16x32_bf16 v[76:79], v[220:223], v[52:55], v[76:79]\n"
        "v_mfma_f32_16x16x32_bf16 v[80:83], v[224:227], v[4:7], v[80:83]\n"
        "ds_read_b128 v[44:47], v3 offset:24576\n"
        "v_mfma_f32_16x16x32_bf16 v[84:87], v[228:231], v[4:7], v[84:87]\n"
        "s_waitcnt lgkmcnt(2)\n"
        "v_mfma_f32_16x16x32_bf16 v[80:83], v[240:243], v[8:11], v[80:83]\n"
        "s_barrier\n"
        "ds_read_b128 v[48:51], v3 offset:26624\n"
        "v_mfma_f32_16x16x32_bf16 v[84:87], v[244:247], v[8:11], v[84:87]\n"
        "v_mfma_f32_16x16x32_bf16 v[80:83], v[208:211], v[12:15], v[80:83]\n"
        "ds_read_b128 v[52:55], v3 offset:28672\n"
        "v_cvt_pk_bf16_f32 v72, v72, v76\n"
        "v_mfma_f32_16x16x32_bf16 v[84:87], v[212:215], v[12:15], v[84:87]\n"
        "v_cvt_pk_bf16_f32 v73, v73, v77\n"
        "v_mfma_f32_16x16x32_bf16 v[80:83], v[232:235], v[16:19], v[80:83]\n"
        "ds_read_b128 v[4:7], v2 offset:30720\n"
        "v_cvt_pk_bf16_f32 v74, v74, v78\n"
        "v_mfma_f32_16x16x32_bf16 v[84:87], v[236:239], v[16:19], v[84:87]\n"
        "v_cvt_pk_bf16_f32 v75, v75, v79\n"
        "v_mfma_f32_16x16x32_bf16 v[80:83], v[248:251], v[20:23], v[80:83]\n"
        "ds_read_b128 v[8:11], v2 offset:32768\n"
        "v_mfma_f32_16x16x32_bf16 v[84:87], v[252:255], v[20:23], v[84:87]\n"
        "ds_write_b128 v1, v[72:75] offset:2048\n"
        "v_mfma_f32_16x16x32_bf16 v[80:83], v[216:219], v[24:27], v[80:83]\n"
        "ds_read_b128 v[12:15], v2 offset:34816\n"
        "v_mfma_f32_16x16x32_bf16 v[84:87], v[220:223], v[24:27], v[84:87]\n"
        "v_mfma_f32_16x16x32_bf16 v[88:91], v[224:227], v[32:35], v[88:91]\n"
        "ds_read_b128 v[16:19], v3 offset:30720\n"
        "v_mfma_f32_16x16x32_bf16 v[92:95], v[228:231], v[32:35], v[92:95]\n"
        "s_waitcnt lgkmcnt(2)\n"
        "v_mfma_f32_16x16x32_bf16 v[88:91], v[240:243], v[36:39], v[88:91]\n"
        "s_barrier\n"
        "ds_read_b128 v[20:23], v3 offset:32768\n"
        "v_mfma_f32_16x16x32_bf16 v[92:95], v[244:247], v[36:39], v[92:95]\n"
        "v_mfma_f32_16x16x32_bf16 v[88:91], v[208:211], v[40:43], v[88:91]\n"
        "ds_read_b128 v[24:27], v3 offset:34816\n"
        "v_cvt_pk_bf16_f32 v80, v80, v84\n"
        "v_mfma_f32_16x16x32_bf16 v[92:95], v[212:215], v[40:43], v[92:95]\n"
        "v_cvt_pk_bf16_f32 v81, v81, v85\n"
        "v_mfma_f32_16x16x32_bf16 v[88:91], v[232:235], v[44:47], v[88:91]\n"
        "ds_read_b128 v[32:35], v2 offset:36864\n"
        "v_cvt_pk_bf16_f32 v82, v82, v86\n"
        "v_mfma_f32_16x16x32_bf16 v[92:95], v[236:239], v[44:47], v[92:95]\n"
        "v_cvt_pk_bf16_f32 v83, v83, v87\n"
        "v_mfma_f32_16x16x32_bf16 v[88:91], v[248:251], v[48:51], v[88:91]\n"
        "ds_read_b128 v[36:39], v2 offset:38912\n"
        "v_mfma_f32_16x16x32_bf16 v[92:95], v[252:255], v[48:51], v[92:95]\n"
        "ds_write_b128 v1, v[80:83] offset:3072\n"
        "v_mfma_f32_16x16x32_bf16 v[88:91], v[216:219], v[52:55], v[88:91]\n"
        "ds_read_b128 v[40:43], v2 offset:40960\n"
        "v_mfma_f32_16x16x32_bf16 v[92:95], v[220:223], v[52:55], v[92:95]\n"
        "v_mfma_f32_16x16x32_bf16 v[96:99], v[224:227], v[4:7], v[96:99]\n"
        "ds_read_b128 v[44:47], v3 offset:36864\n"
        "v_mfma_f32_16x16x32_bf16 v[100:103], v[228:231], v[4:7], v[100:103]\n"
        "s_waitcnt lgkmcnt(2)\n"
        "v_mfma_f32_16x16x32_bf16 v[96:99], v[240:243], v[8:11], v[96:99]\n"
        "s_barrier\n"
        "ds_read_b128 v[48:51], v3 offset:38912\n"
        "v_mfma_f32_16x16x32_bf16 v[100:103], v[244:247], v[8:11], v[100:103]\n"
        "v_mfma_f32_16x16x32_bf16 v[96:99], v[208:211], v[12:15], v[96:99]\n"
        "ds_read_b128 v[52:55], v3 offset:40960\n"
        "v_cvt_pk_bf16_f32 v88, v88, v92\n"
        "v_mfma_f32_16x16x32_bf16 v[100:103], v[212:215], v[12:15], v[100:103]\n"
        "v_cvt_pk_bf16_f32 v89, v89, v93\n"
        "v_mfma_f32_16x16x32_bf16 v[96:99], v[232:235], v[16:19], v[96:99]\n"
        "ds_read_b128 v[4:7], v2 offset:43008\n"
        "v_cvt_pk_bf16_f32 v90, v90, v94\n"
        "v_mfma_f32_16x16x32_bf16 v[100:103], v[236:239], v[16:19], v[100:103]\n"
        "v_cvt_pk_bf16_f32 v91, v91, v95\n"
        "v_mfma_f32_16x16x32_bf16 v[96:99], v[248:251], v[20:23], v[96:99]\n"
        "ds_read_b128 v[8:11], v2 offset:45056\n"
        "v_mfma_f32_16x16x32_bf16 v[100:103], v[252:255], v[20:23], v[100:103]\n"
        "ds_write_b128 v1, v[88:91] offset:4096\n"
        "v_mfma_f32_16x16x32_bf16 v[96:99], v[216:219], v[24:27], v[96:99]\n"
        "ds_read_b128 v[12:15], v2 offset:47104\n"
        "v_mfma_f32_16x16x32_bf16 v[100:103], v[220:223], v[24:27], v[100:103]\n"
        "v_mfma_f32_16x16x32_bf16 v[104:107], v[224:227], v[32:35], v[104:107]\n"
        "ds_read_b128 v[16:19], v3 offset:43008\n"
        "v_mfma_f32_16x16x32_bf16 v[108:111], v[228:231], v[32:35], v[108:111]\n"
        "s_waitcnt lgkmcnt(2)\n"
        "v_mfma_f32_16x16x32_bf16 v[104:107], v[240:243], v[36:39], v[104:107]\n"
        "s_barrier\n"
        "ds_read_b128 v[20:23], v3 offset:45056\n"
        "v_mfma_f32_16x16x32_bf16 v[108:111], v[244:247], v[36:39], v[108:111]\n"
        "v_mfma_f32_16x16x32_bf16 v[104:107], v[208:211], v[40:43], v[104:107]\n"
        "ds_read_b128 v[24:27], v3 offset:47104\n"
        "v_cvt_pk_bf16_f32 v96, v96, v100\n"
        "v_mfma_f32_16x16x32_bf16 v[108:111], v[212:215], v[40:43], v[108:111]\n"
        "v_cvt_pk_bf16_f32 v97, v97, v101\n"
        "v_mfma_f32_16x16x32_bf16 v[104:107], v[232:235], v[44:47], v[104:107]\n"
        "ds_read_b128 v[32:35], v2 offset:49152\n"
        "v_cvt_pk_bf16_f32 v98, v98, v102\n"
        "v_mfma_f32_16x16x32_bf16 v[108:111], v[236:239], v[44:47], v[108:111]\n"
        "v_cvt_pk_bf16_f32 v99, v99, v103\n"
        "v_mfma_f32_16x16x32_bf16 v[104:107], v[248:251], v[48:51], v[104:107]\n"
        "ds_read_b128 v[36:39], v2 offset:51200\n"
        "v_mfma_f32_16x16x32_bf16 v[108:111], v[252:255], v[48:51], v[108:111]\n"
        "ds_write_b128 v1, v[96:99] offset:5120\n"
        "v_mfma_f32_16x16x32_bf16 v[104:107], v[216:219], v[52:55], v[104:107]\n"
        "ds_read_b128 v[40:43], v2 offset:53248\n"
        "v_mfma_f32_16x16x32_bf16 v[108:111], v[220:223], v[52:55], v[108:111]\n"
        "v_mfma_f32_16x16x32_bf16 v[112:115], v[224:227], v[4:7], v[112:115]\n"
        "ds_read_b128 v[44:47], v3 offset:49152\n"
        "v_mfma_f32_16x16x32_bf16 v[116:119], v[228:231], v[4:7], v[116:119]\n"
        "s_waitcnt lgkmcnt(2)\n"
        "v_mfma_f32_16x16x32_bf16 v[112:115], v[240:243], v[8:11], v[112:115]\n"
        "s_barrier\n"
        "ds_read_b128 v[48:51], v3 offset:51200\n"
        "v_mfma_f32_16x16x32_bf16 v[116:119], v[244:247], v[8:11], v[116:119]\n"
        "v_mfma_f32_16x16x32_bf16 v[112:115], v[208:211], v[12:15], v[112:115]\n"
        "ds_read_b128 v[52:55], v3 offset:53248\n"
        "v_cvt_pk_bf16_f32 v104, v104, v108\n"
        "v_mfma_f32_16x16x32_bf16 v[116:119], v[212:215], v[12:15], v[116:119]\n"
        "v_cvt_pk_bf16_f32 v105, v105, v109\n"
        "v_mfma_f32_16x16x32_bf16 v[112:115], v[232:235], v[16:19], v[112:115]\n"
        "ds_read_b128 v[4:7], v2 offset:55296\n"
        "v_cvt_pk_bf16_f32 v106, v106, v110\n"
        "v_mfma_f32_16x16x32_bf16 v[116:119], v[236:239], v[16:19], v[116:119]\n"
        "v_cvt_pk_bf16_f32 v107, v107, v111\n"
        "v_mfma_f32_16x16x32_bf16 v[112:115], v[248:251], v[20:23], v[112:115]\n"
        "ds_read_b128 v[8:11], v2 offset:57344\n"
        "v_mfma_f32_16x16x32_bf16 v[116:119], v[252:255], v[20:23], v[116:119]\n"
        "ds_write_b128 v1, v[104:107] offset:6144\n"
        "v_mfma_f32_16x16x32_bf16 v[112:115], v[216:219], v[24:27], v[112:115]\n"
        "ds_read_b128 v[12:15], v2 offset:59392\n"
        "v_mfma_f32_16x16x32_bf16 v[116:119], v[220:223], v[24:27], v[116:119]\n"
        "v_add_u32 v2, 61440, v2\n"
        "v_mfma_f32_16x16x32_bf16 v[120:123], v[224:227], v[32:35], v[120:123]\n"
        "ds_read_b128 v[16:19], v3 offset:55296\n"
        "v_mfma_f32_16x16x32_bf16 v[124:127], v[228:231], v[32:35], v[124:127]\n"
        "s_waitcnt lgkmcnt(2)\n"
        "v_mfma_f32_16x16x32_bf16 v[120:123], v[240:243], v[36:39], v[120:123]\n"
        "s_barrier\n"
        "ds_read_b128 v[20:23], v3 offset:57344\n"
        "v_mfma_f32_16x16x32_bf16 v[124:127], v[244:247], v[36:39], v[124:127]\n"
        "v_mfma_f32_16x16x32_bf16 v[120:123], v[208:211], v[40:43], v[120:123]\n"
        "ds_read_b128 v[24:27], v3 offset:59392\n"
        "v_cvt_pk_bf16_f32 v112, v112, v116\n"
        "v_mfma_f32_16x16x32_bf16 v[124:127], v[212:215], v[40:43], v[124:127]\n"
        "v_cvt_pk_bf16_f32 v113, v113, v117\n"
        "v_add_u32 v3, 61440, v3\n"
        "v_mfma_f32_16x16x32_bf16 v[120:123], v[232:235], v[44:47], v[120:123]\n"
        "ds_read_b128 v[32:35], v2 offset:0\n"
        "v_cvt_pk_bf16_f32 v114, v114, v118\n"
        "v_mfma_f32_16x16x32_bf16 v[124:127], v[236:239], v[44:47], v[124:127]\n"
        "v_cvt_pk_bf16_f32 v115, v115, v119\n"
        "v_mfma_f32_16x16x32_bf16 v[120:123], v[248:251], v[48:51], v[120:123]\n"
        "ds_read_b128 v[36:39], v2 offset:2048\n"
        "v_mfma_f32_16x16x32_bf16 v[124:127], v[252:255], v[48:51], v[124:127]\n"
        "ds_write_b128 v1, v[112:115] offset:7168\n"
        "v_mfma_f32_16x16x32_bf16 v[120:123], v[216:219], v[52:55], v[120:123]\n"
        "ds_read_b128 v[40:43], v2 offset:4096\n"
        "v_mfma_f32_16x16x32_bf16 v[124:127], v[220:223], v[52:55], v[124:127]\n"
        "v_mfma_f32_16x16x32_bf16 v[128:131], v[224:227], v[4:7], v[128:131]\n"
        "ds_read_b128 v[44:47], v3 offset:0\n"
        "v_mfma_f32_16x16x32_bf16 v[132:135], v[228:231], v[4:7], v[132:135]\n"
        "s_waitcnt lgkmcnt(2)\n"
        "v_mfma_f32_16x16x32_bf16 v[128:131], v[240:243], v[8:11], v[128:131]\n"
        "s_barrier\n"
        "ds_read_b128 v[48:51], v3 offset:2048\n"
        "v_mfma_f32_16x16x32_bf16 v[132:135], v[244:247], v[8:11], v[132:135]\n"
        "v_mfma_f32_16x16x32_bf16 v[128:131], v[208:211], v[12:15], v[128:131]\n"
        "ds_read_b128 v[52:55], v3 offset:4096\n"
        "v_cvt_pk_bf16_f32 v120, v120, v124\n"
        "v_mfma_f32_16x16x32_bf16 v[132:135], v[212:215], v[12:15], v[132:135]\n"
        "v_cvt_pk_bf16_f32 v121, v121, v125\n"
        "v_mfma_f32_16x16x32_bf16 v[128:131], v[232:235], v[16:19], v[128:131]\n"
        "ds_read_b128 v[4:7], v2 offset:6144\n"
        "v_cvt_pk_bf16_f32 v122, v122, v126\n"
        "v_mfma_f32_16x16x32_bf16 v[132:135], v[236:239], v[16:19], v[132:135]\n"
        "v_cvt_pk_bf16_f32 v123, v123, v127\n"
        "v_mfma_f32_16x16x32_bf16 v[128:131], v[248:251], v[20:23], v[128:131]\n"
        "ds_read_b128 v[8:11], v2 offset:8192\n"
        "v_mfma_f32_16x16x32_bf16 v[132:135], v[252:255], v[20:23], v[132:135]\n"
        "ds_write_b128 v1, v[120:123] offset:0\n"
        "v_mfma_f32_16x16x32_bf16 v[128:131], v[216:219], v[24:27], v[128:131]\n"
        "ds_read_b128 v[12:15], v2 offset:10240\n"
        "v_mfma_f32_16x16x32_bf16 v[132:135], v[220:223], v[24:27], v[132:135]\n"
        "v_mfma_f32_16x16x32_bf16 v[136:139], v[224:227], v[32:35], v[136:139]\n"
        "ds_read_b128 v[16:19], v3 offset:6144\n"
        "v_mfma_f32_16x16x32_bf16 v[140:143], v[228:231], v[32:35], v[140:143]\n"
        "s_waitcnt lgkmcnt(2)\n"
        "v_mfma_f32_16x16x32_bf16 v[136:139], v[240:243], v[36:39], v[136:139]\n"
        "s_barrier\n"
        "ds_read_b128 v[20:23], v3 offset:8192\n"
        "v_mfma_f32_16x16x32_bf16 v[140:143], v[244:247], v[36:39], v[140:143]\n"
        "v_mfma_f32_16x16x32_bf16 v[136:139], v[208:211], v[40:43], v[136:139]\n"
        "ds_read_b128 v[24:27], v3 offset:10240\n"
        "v_cvt_pk_bf16_f32 v128, v128, v132\n"
        "v_mfma_f32_16x16x32_bf16 v[140:143], v[212:215], v[40:43], v[140:143]\n"
        "v_cvt_pk_bf16_f32 v129, v129, v133\n"
        "v_mfma_f32_16x16x32_bf16 v[136:139], v[232:235], v[44:47], v[136:139]\n"
        "ds_read_b128 v[32:35], v2 offset:12288\n"
        "v_cvt_pk_bf16_f32 v130, v130, v134\n"
        "v_mfma_f32_16x16x32_bf16 v[140:143], v[236:239], v[44:47], v[140:143]\n"
        "v_cvt_pk_bf16_f32 v131, v131, v135\n"
        "v_mfma_f32_16x16x32_bf16 v[136:139], v[248:251], v[48:51], v[136:139]\n"
        "ds_read_b128 v[36:39], v2 offset:14336\n"
        "v_mfma_f32_16x16x32_bf16 v[140:143], v[252:255], v[48:51], v[140:143]\n"
        "ds_write_b128 v1, v[128:131] offset:1024\n"
        "v_mfma_f32_16x16x32_bf16 v[136:139], v[216:219], v[52:55], v[136:139]\n"
        "ds_read_b128 v[40:43], v2 offset:16384\n"
        "v_mfma_f32_16x16x32_bf16 v[140:143], v[220:223], v[52:55], v[140:143]\n"
        "v_mfma_f32_16x16x32_bf16 v[144:147], v[224:227], v[4:7], v[144:147]\n"
        "ds_read_b128 v[44:47], v3 offset:12288\n"
        "v_mfma_f32_16x16x32_bf16 v[148:151], v[228:231], v[4:7], v[148:151]\n"
        "s_waitcnt lgkmcnt(2)\n"
        "v_mfma_f32_16x16x32_bf16 v[144:147], v[240:243], v[8:11], v[144:147]\n"
        "s_barrier\n"
        "ds_read_b128 v[48:51], v3 offset:14336\n"
        "v_mfma_f32_16x16x32_bf16 v[148:151], v[244:247], v[8:11], v[148:151]\n"
        "v_mfma_f32_16x16x32_bf16 v[144:147], v[208:211], v[12:15], v[144:147]\n"
        "ds_read_b128 v[52:55], v3 offset:16384\n"
        "v_cvt_pk_bf16_f32 v136, v136, v140\n"
        "v_mfma_f32_16x16x32_bf16 v[148:151], v[212:215], v[12:15], v[148:151]\n"
        "v_cvt_pk_bf16_f32 v137, v137, v141\n"
        "v_mfma_f32_16x16x32_bf16 v[144:147], v[232:235], v[16:19], v[144:147]\n"
        "ds_read_b128 v[4:7], v2 offset:18432\n"
        "v_cvt_pk_bf16_f32 v138, v138, v142\n"
        "v_mfma_f32_16x16x32_bf16 v[148:151], v[236:239], v[16:19], v[148:151]\n"
        "v_cvt_pk_bf16_f32 v139, v139, v143\n"
        "v_mfma_f32_16x16x32_bf16 v[144:147], v[248:251], v[20:23], v[144:147]\n"
        "ds_read_b128 v[8:11], v2 offset:20480\n"
        "v_mfma_f32_16x16x32_bf16 v[148:151], v[252:255], v[20:23], v[148:151]\n"
        "ds_write_b128 v1, v[136:139] offset:2048\n"
        "v_mfma_f32_16x16x32_bf16 v[144:147], v[216:219], v[24:27], v[144:147]\n"
        "ds_read_b128 v[12:15], v2 offset:22528\n"
        "v_mfma_f32_16x16x32_bf16 v[148:151], v[220:223], v[24:27], v[148:151]\n"
        "v_mfma_f32_16x16x32_bf16 v[152:155], v[224:227], v[32:35], v[152:155]\n"
        "ds_read_b128 v[16:19], v3 offset:18432\n"
        "v_mfma_f32_16x16x32_bf16 v[156:159], v[228:231], v[32:35], v[156:159]\n"
        "s_waitcnt lgkmcnt(2)\n"
        "v_mfma_f32_16x16x32_bf16 v[152:155], v[240:243], v[36:39], v[152:155]\n"
        "s_barrier\n"
        "ds_read_b128 v[20:23], v3 offset:20480\n"
        "v_mfma_f32_16x16x32_bf16 v[156:159], v[244:247], v[36:39], v[156:159]\n"
        "v_mfma_f32_16x16x32_bf16 v[152:155], v[208:211], v[40:43], v[152:155]\n"
        "ds_read_b128 v[24:27], v3 offset:22528\n"
        "v_cvt_pk_bf16_f32 v144, v144, v148\n"
        "v_mfma_f32_16x16x32_bf16 v[156:159], v[212:215], v[40:43], v[156:159]\n"
        "v_cvt_pk_bf16_f32 v145, v145, v149\n"
        "v_mfma_f32_16x16x32_bf16 v[152:155], v[232:235], v[44:47], v[152:155]\n"
        "ds_read_b128 v[32:35], v2 offset:24576\n"
        "v_cvt_pk_bf16_f32 v146, v146, v150\n"
        "v_mfma_f32_16x16x32_bf16 v[156:159], v[236:239], v[44:47], v[156:159]\n"
        "v_cvt_pk_bf16_f32 v147, v147, v151\n"
        "v_mfma_f32_16x16x32_bf16 v[152:155], v[248:251], v[48:51], v[152:155]\n"
        "ds_read_b128 v[36:39], v2 offset:26624\n"
        "v_mfma_f32_16x16x32_bf16 v[156:159], v[252:255], v[48:51], v[156:159]\n"
        "ds_write_b128 v1, v[144:147] offset:3072\n"
        "v_mfma_f32_16x16x32_bf16 v[152:155], v[216:219], v[52:55], v[152:155]\n"
        "ds_read_b128 v[40:43], v2 offset:28672\n"
        "v_mfma_f32_16x16x32_bf16 v[156:159], v[220:223], v[52:55], v[156:159]\n"
        "v_mfma_f32_16x16x32_bf16 v[160:163], v[224:227], v[4:7], v[160:163]\n"
        "ds_read_b128 v[44:47], v3 offset:24576\n"
        "v_mfma_f32_16x16x32_bf16 v[164:167], v[228:231], v[4:7], v[164:167]\n"
        "s_waitcnt lgkmcnt(2)\n"
        "v_mfma_f32_16x16x32_bf16 v[160:163], v[240:243], v[8:11], v[160:163]\n"
        "s_barrier\n"
        "ds_read_b128 v[48:51], v3 offset:26624\n"
        "v_mfma_f32_16x16x32_bf16 v[164:167], v[244:247], v[8:11], v[164:167]\n"
        "v_mfma_f32_16x16x32_bf16 v[160:163], v[208:211], v[12:15], v[160:163]\n"
        "ds_read_b128 v[52:55], v3 offset:28672\n"
        "v_cvt_pk_bf16_f32 v152, v152, v156\n"
        "v_mfma_f32_16x16x32_bf16 v[164:167], v[212:215], v[12:15], v[164:167]\n"
        "v_cvt_pk_bf16_f32 v153, v153, v157\n"
        "v_mfma_f32_16x16x32_bf16 v[160:163], v[232:235], v[16:19], v[160:163]\n"
        "ds_read_b128 v[4:7], v2 offset:30720\n"
        "v_cvt_pk_bf16_f32 v154, v154, v158\n"
        "v_mfma_f32_16x16x32_bf16 v[164:167], v[236:239], v[16:19], v[164:167]\n"
        "v_cvt_pk_bf16_f32 v155, v155, v159\n"
        "v_mfma_f32_16x16x32_bf16 v[160:163], v[248:251], v[20:23], v[160:163]\n"
        "ds_read_b128 v[8:11], v2 offset:32768\n"
        "v_mfma_f32_16x16x32_bf16 v[164:167], v[252:255], v[20:23], v[164:167]\n"
        "ds_write_b128 v1, v[152:155] offset:4096\n"
        "v_mfma_f32_16x16x32_bf16 v[160:163], v[216:219], v[24:27], v[160:163]\n"
        "ds_read_b128 v[12:15], v2 offset:34816\n"
        "v_mfma_f32_16x16x32_bf16 v[164:167], v[220:223], v[24:27], v[164:167]\n"
        "v_mfma_f32_16x16x32_bf16 v[168:171], v[224:227], v[32:35], v[168:171]\n"
        "ds_read_b128 v[16:19], v3 offset:30720\n"
        "v_mfma_f32_16x16x32_bf16 v[172:175], v[228:231], v[32:35], v[172:175]\n"
        "s_waitcnt lgkmcnt(2)\n"
        "v_mfma_f32_16x16x32_bf16 v[168:171], v[240:243], v[36:39], v[168:171]\n"
        "s_barrier\n"
        "ds_read_b128 v[20:23], v3 offset:32768\n"
        "v_mfma_f32_16x16x32_bf16 v[172:175], v[244:247], v[36:39], v[172:175]\n"
        "v_mfma_f32_16x16x32_bf16 v[168:171], v[208:211], v[40:43], v[168:171]\n"
        "ds_read_b128 v[24:27], v3 offset:34816\n"
        "v_cvt_pk_bf16_f32 v160, v160, v164\n"
        "v_mfma_f32_16x16x32_bf16 v[172:175], v[212:215], v[40:43], v[172:175]\n"
        "v_cvt_pk_bf16_f32 v161, v161, v165\n"
        "v_mfma_f32_16x16x32_bf16 v[168:171], v[232:235], v[44:47], v[168:171]\n"
        "ds_read_b128 v[32:35], v2 offset:36864\n"
        "v_cvt_pk_bf16_f32 v162, v162, v166\n"
        "v_mfma_f32_16x16x32_bf16 v[172:175], v[236:239], v[44:47], v[172:175]\n"
        "v_cvt_pk_bf16_f32 v163, v163, v167\n"
        "v_mfma_f32_16x16x32_bf16 v[168:171], v[248:251], v[48:51], v[168:171]\n"
        "ds_read_b128 v[36:39], v2 offset:38912\n"
        "v_mfma_f32_16x16x32_bf16 v[172:175], v[252:255], v[48:51], v[172:175]\n"
        "ds_write_b128 v1, v[160:163] offset:5120\n"
        "v_mfma_f32_16x16x32_bf16 v[168:171], v[216:219], v[52:55], v[168:171]\n"
        "ds_read_b128 v[40:43], v2 offset:40960\n"
        "v_mfma_f32_16x16x32_bf16 v[172:175], v[220:223], v[52:55], v[172:175]\n"
        "v_mfma_f32_16x16x32_bf16 v[176:179], v[224:227], v[4:7], v[176:179]\n"
        "ds_read_b128 v[44:47], v3 offset:36864\n"
        "v_mfma_f32_16x16x32_bf16 v[180:183], v[228:231], v[4:7], v[180:183]\n"
        "s_waitcnt lgkmcnt(2)\n"
        "v_mfma_f32_16x16x32_bf16 v[176:179], v[240:243], v[8:11], v[176:179]\n"
        "s_barrier\n"
        "ds_read_b128 v[48:51], v3 offset:38912\n"
        "v_mfma_f32_16x16x32_bf16 v[180:183], v[244:247], v[8:11], v[180:183]\n"
        "v_mfma_f32_16x16x32_bf16 v[176:179], v[208:211], v[12:15], v[176:179]\n"
        "ds_read_b128 v[52:55], v3 offset:40960\n"
        "v_cvt_pk_bf16_f32 v168, v168, v172\n"
        "v_mfma_f32_16x16x32_bf16 v[180:183], v[212:215], v[12:15], v[180:183]\n"
        "v_cvt_pk_bf16_f32 v169, v169, v173\n"
        "v_mfma_f32_16x16x32_bf16 v[176:179], v[232:235], v[16:19], v[176:179]\n"
        "ds_read_b128 v[4:7], v2 offset:43008\n"
        "v_cvt_pk_bf16_f32 v170, v170, v174\n"
        "v_mfma_f32_16x16x32_bf16 v[180:183], v[236:239], v[16:19], v[180:183]\n"
        "v_cvt_pk_bf16_f32 v171, v171, v175\n"
        "v_mfma_f32_16x16x32_bf16 v[176:179], v[248:251], v[20:23], v[176:179]\n"
        "ds_read_b128 v[8:11], v2 offset:45056\n"
        "v_mfma_f32_16x16x32_bf16 v[180:183], v[252:255], v[20:23], v[180:183]\n"
        "ds_write_b128 v1, v[168:171] offset:6144\n"
        "v_mfma_f32_16x16x32_bf16 v[176:179], v[216:219], v[24:27], v[176:179]\n"
        "ds_read_b128 v[12:15], v2 offset:47104\n"
        "v_mfma_f32_16x16x32_bf16 v[180:183], v[220:223], v[24:27], v[180:183]\n"
        "v_mfma_f32_16x16x32_bf16 v[184:187], v[224:227], v[32:35], v[184:187]\n"
        "ds_read_b128 v[16:19], v3 offset:43008\n"
        "v_mfma_f32_16x16x32_bf16 v[188:191], v[228:231], v[32:35], v[188:191]\n"
        "s_waitcnt lgkmcnt(2)\n"
        "v_mfma_f32_16x16x32_bf16 v[184:187], v[240:243], v[36:39], v[184:187]\n"
        "s_barrier\n"
        "ds_read_b128 v[20:23], v3 offset:45056\n"
        "v_mfma_f32_16x16x32_bf16 v[188:191], v[244:247], v[36:39], v[188:191]\n"
        "v_mfma_f32_16x16x32_bf16 v[184:187], v[208:211], v[40:43], v[184:187]\n"
        "ds_read_b128 v[24:27], v3 offset:47104\n"
        "v_cvt_pk_bf16_f32 v176, v176, v180\n"
        "v_mfma_f32_16x16x32_bf16 v[188:191], v[212:215], v[40:43], v[188:191]\n"
        "v_cvt_pk_bf16_f32 v177, v177, v181\n"
        "v_mfma_f32_16x16x32_bf16 v[184:187], v[232:235], v[44:47], v[184:187]\n"
        "ds_read_b128 v[32:35], v2 offset:49152\n"
        "v_cvt_pk_bf16_f32 v178, v178, v182\n"
        "v_mfma_f32_16x16x32_bf16 v[188:191], v[236:239], v[44:47], v[188:191]\n"
        "v_cvt_pk_bf16_f32 v179, v179, v183\n"
        "v_mfma_f32_16x16x32_bf16 v[184:187], v[248:251], v[48:51], v[184:187]\n"
        "ds_read_b128 v[36:39], v2 offset:51200\n"
        "v_mfma_f32_16x16x32_bf16 v[188:191], v[252:255], v[48:51], v[188:191]\n"
        "ds_write_b128 v1, v[176:179] offset:7168\n"
        "v_mfma_f32_16x16x32_bf16 v[184:187], v[216:219], v[52:55], v[184:187]\n"
        "ds_read_b128 v[40:43], v2 offset:53248\n"
        "v_mfma_f32_16x16x32_bf16 v[188:191], v[220:223], v[52:55], v[188:191]\n"
        "v_mfma_f32_16x16x32_bf16 v[192:195], v[224:227], v[4:7], v[192:195]\n"
        "ds_read_b128 v[44:47], v3 offset:49152\n"
        "v_mfma_f32_16x16x32_bf16 v[196:199], v[228:231], v[4:7], v[196:199]\n"
        "s_waitcnt lgkmcnt(2)\n"
        "v_mfma_f32_16x16x32_bf16 v[192:195], v[240:243], v[8:11], v[192:195]\n"
        "s_barrier\n"
        "ds_read_b128 v[48:51], v3 offset:51200\n"
        "v_mfma_f32_16x16x32_bf16 v[196:199], v[244:247], v[8:11], v[196:199]\n"
        "v_mfma_f32_16x16x32_bf16 v[192:195], v[208:211], v[12:15], v[192:195]\n"
        "ds_read_b128 v[52:55], v3 offset:53248\n"
        "v_cvt_pk_bf16_f32 v184, v184, v188\n"
        "v_mfma_f32_16x16x32_bf16 v[196:199], v[212:215], v[12:15], v[196:199]\n"
        "v_cvt_pk_bf16_f32 v185, v185, v189\n"
        "v_mfma_f32_16x16x32_bf16 v[192:195], v[232:235], v[16:19], v[192:195]\n"
        "v_cvt_pk_bf16_f32 v186, v186, v190\n"
        "v_mfma_f32_16x16x32_bf16 v[196:199], v[236:239], v[16:19], v[196:199]\n"
        "v_cvt_pk_bf16_f32 v187, v187, v191\n"
        "v_mfma_f32_16x16x32_bf16 v[192:195], v[248:251], v[20:23], v[192:195]\n"
        "v_mfma_f32_16x16x32_bf16 v[196:199], v[252:255], v[20:23], v[196:199]\n"
        "ds_write_b128 v1, v[184:187] offset:0\n"
        "v_mfma_f32_16x16x32_bf16 v[192:195], v[216:219], v[24:27], v[192:195]\n"
        "v_mfma_f32_16x16x32_bf16 v[196:199], v[220:223], v[24:27], v[196:199]\n"
        "s_cbranch_vccnz .Lsq_e17\n"
        "v_mfma_f32_16x16x32_bf16 v[200:203], v[224:227], v[32:35], v[200:203]\n"
        "v_mfma_f32_16x16x32_bf16 v[204:207], v[228:231], v[32:35], v[204:207]\n"
        "s_waitcnt lgkmcnt(0)\n"
        "v_mfma_f32_16x16x32_bf16 v[200:203], v[240:243], v[36:39], v[200:203]\n"
        "s_barrier\n"
        "v_mfma_f32_16x16x32_bf16 v[204:207], v[244:247], v[36:39], v[204:207]\n"
        "v_mfma_f32_16x16x32_bf16 v[200:203], v[208:211], v[40:43], v[200:203]\n"
        "v_cvt_pk_bf16_f32 v192, v192, v196\n"
        "v_mfma_f32_16x16x32_bf16 v[204:207], v[212:215], v[40:43], v[204:207]\n"
        "v_cvt_pk_bf16_f32 v193, v193, v197\n"
        "v_mfma_f32_16x16x32_bf16 v[200:203], v[232:235], v[44:47], v[200:203]\n"
        "v_cvt_pk_bf16_f32 v194, v194, v198\n"
        "v_mfma_f32_16x16x32_bf16 v[204:207], v[236:239], v[44:47], v[204:207]\n"
        "v_cvt_pk_bf16_f32 v195, v195, v199\n"
        "v_mfma_f32_16x16x32_bf16 v[200:203], v[248:251], v[48:51], v[200:203]\n"
        "v_mfma_f32_16x16x32_bf16 v[204:207], v[252:255], v[48:51], v[204:207]\n"
        "ds_write_b128 v1, v[192:195] offset:1024\n"
        "v_mfma_f32_16x16x32_bf16 v[200:203], v[216:219], v[52:55], v[200:203]\n"
        "v_mfma_f32_16x16x32_bf16 v[204:207], v[220:223], v[52:55], v[204:207]\n"
        "s_nop 7\n"
        "v_cvt_pk_bf16_f32 v200, v200, v204\n"
        "v_cvt_pk_bf16_f32 v201, v201, v205\n"
        "v_cvt_pk_bf16_f32 v202, v202, v206\n"
        "v_cvt_pk_bf16_f32 v203, v203, v207\n"
        "ds_write_b128 v1, v[200:203] offset:2048\n"
        "s_waitcnt lgkmcnt(0)\n"
        "s_barrier\n"
        "s_endpgm\n"
        ".Lsq_e17:\n"
        "s_nop 7\n"
        "v_cvt_pk_bf16_f32 v192, v192, v196\n"
        "v_cvt_pk_bf16_f32 v193, v193, v197\n"
        "v_cvt_pk_bf16_f32 v194, v194, v198\n"
        "v_cvt_pk_bf16_f32 v195, v195, v199\n"
        "ds_write_b128 v1, v[192:195] offset:1024\n"
        "s_waitcnt lgkmcnt(0)\n"
        "s_barrier\n"
        "s_endpgm\n"
        ".p2align 3\n"
        ".Lsq_dq0:\n"
        "ds_read_b128 v[20:23], v0 offset:0\n"
        "ds_read_b128 v[24:27], v0 offset:2048\n"
        "v_add_u32 v0, s33, v0\n"
        "s_sub_u32 s33, 0, s33\n"
        "s_waitcnt lgkmcnt(0)\n"
        "s_nop 0\n"
        "v_perm_b32 v28, v5, v4, v20\n"
        "v_perm_b32 v30, v9, v8, v20\n"
        "v_xor_b32_e64 v20, s44, v20\n"
        "v_perm_b32 v29, v7, v6, v20\n"
        "v_perm_b32 v31, v11, v10, v20\n"
        "v_xor_b32 v28, v28, v29\n"
        "v_xor_b32 v30, v30, v31\n"
        "v_perm_b32 v208, v30, v28, s40\n"
        "v_perm_b32 v209, v30, v28, s41\n"
        "v_perm_b32 v28, v5, v4, v21\n"
        "v_perm_b32 v30, v9, v8, v21\n"
        "v_xor_b32_e64 v21, s44, v21\n"
        "v_perm_b32 v29, v7, v6, v21\n"
        "v_perm_b32 v31, v11, v10, v21\n"
        "v_xor_b32 v28, v28, v29\n"
        "v_xor_b32 v30, v30, v31\n"
        "v_perm_b32 v210, v30, v28, s40\n"
        "v_perm_b32 v211, v30, v28, s41\n"
        "v_perm_b32 v28, v13, v12, v22\n"
        "v_perm_b32 v30, v17, v16, v22\n"
        "v_xor_b32_e64 v22, s44, v22\n"
        "v_perm_b32 v29, v15, v14, v22\n"
        "v_perm_b32 v31, v19, v18, v22\n"
        "v_xor_b32 v28, v28, v29\n"
        "v_xor_b32 v30, v30, v31\n"
        "v_perm_b32 v212, v30, v28, s40\n"
        "v_perm_b32 v213, v30, v28, s41\n"
        "v_perm_b32 v28, v13, v12, v23\n"
        "v_perm_b32 v30, v17, v16, v23\n"
        "v_xor_b32_e64 v23, s44, v23\n"
        "v_perm_b32 v29, v15, v14, v23\n"
        "v_perm_b32 v31, v19, v18, v23\n"
        "v_xor_b32 v28, v28, v29\n"
        "v_xor_b32 v30, v30, v31\n"
        "v_perm_b32 v214, v30, v28, s40\n"
        "v_perm_b32 v215, v30, v28, s41\n"
        "s_setpc_b64 s[56:57]\n"
        ".p2align 3\n"
        ".Lsq_dq1:\n"
        "v_perm_b32 v28, v5, v4, v24\n"
        "v_perm_b32 v30, v9, v8, v24\n"
        "v_xor_b32_e64 v24, s44, v24\n"
        "v_perm_b32 v29, v7, v6, v24\n"
        "v_perm_b32 v31, v11, v10, v24\n"
        "v_xor_b32 v28, v28, v29\n"
        "v_xor_b32 v30, v30, v31\n"
        "v_perm_b32 v216, v30, v28, s40\n"
        "v_perm_b32 v217, v30, v28, s41\n"
        "v_perm_b32 v28, v5, v4, v25\n"
        "v_perm_b32 v30, v9, v8, v25\n"
        "v_xor_b32_e64 v25, s44, v25\n"
        "v_perm_b32 v29, v7, v6, v25\n"
        "v_perm_b32 v31, v11, v10, v25\n"
        "v_xor_b32 v28, v28, v29\n"
        "v_xor_b32 v30, v30, v31\n"
        "v_perm_b32 v218, v30, v28, s40\n"
        "v_perm_b32 v219, v30, v28, s41\n"
        "v_perm_b32 v28, v13, v12, v26\n"
        "v_perm_b32 v30, v17, v16, v26\n"
        "v_xor_b32_e64 v26, s44, v26\n"
        "v_perm_b32 v29, v15, v14, v26\n"
        "v_perm_b32 v31, v19, v18, v26\n"
        "v_xor_b32 v28, v28, v29\n"
        "v_xor_b32 v30, v30, v31\n"
        "v_perm_b32 v220, v30, v28, s40\n"
        "v_perm_b32 v221, v30, v28, s41\n"
        "v_perm_b32 v28, v13, v12, v27\n"
        "v_perm_b32 v30, v17, v16, v27\n"
        "v_xor_b32_e64 v27, s44, v27\n"
        "v_perm_b32 v29, v15, v14, v27\n"
        "v_perm_b32 v31, v19, v18, v27\n"
        "v_xor_b32 v28, v28, v29\n"
        "v_xor_b32 v30, v30, v31\n"
        "v_perm_b32 v222, v30, v28, s40\n"
        "v_perm_b32 v223, v30, v28, s41\n"
        "s_setpc_b64 s[56:57]\n"
        ".Lsq_end:\n"
        :: "s"(__builtin_amdgcn_kernarg_segment_ptr()), "s"(__builtin_amdgcn_workgroup_id_x()), "v"(__builtin_amdgcn_workitem_id_x())
        : "v1", "v2", "v3", "v4", "v5", "v6", "v7", "v8", "v9", "v10", "v11", "v12", "v13", "v14", "v15", "v16", "v17", "v18", "v19", "v20", "v21", "v22", "v23", "v24", "v25", "v26", "v27", "v28", "v29", "v30", "v31", "v32", "v33", "v34", "v35", "v36", "v37", "v38", "v39", "v40", "v41", "v42", "v43", "v44", "v45", "v46", "v47", "v48", "v49", "v50", "v51", "v52", "v53", "v54", "v55", "v56", "v57", "v58", "v59", "v60", "v61", "v62", "v63", "v64", "v65", "v66", "v67", "v68", "v69", "v70", "v71", "v72", "v73", "v74", "v75", "v76", "v77", "v78", "v79", "v80", "v81", "v82", "v83", "v84", "v85", "v86", "v87", "v88", "v89", "v90", "v91", "v92", "v93", "v94", "v95", "v96", "v97", "v98", "v99", "v100", "v101", "v102", "v103", "v104", "v105", "v106", "v107", "v108", "v109", "v110", "v111", "v112", "v113", "v114", "v115", "v116", "v117", "v118", "v119", "v120", "v121", "v122", "v123", "v124", "v125", "v126", "v127", "v128", "v129", "v130", "v131", "v132", "v133", "v134", "v135", "v136", "v137", "v138", "v139", "v140", "v141", "v142", "v143", "v144", "v145", "v146", "v147", "v148", "v149", "v150", "v151", "v152", "v153", "v154", "v155", "v156", "v157", "v158", "v159", "v160", "v161", "v162", "v163", "v164", "v165", "v166", "v167", "v168", "v169", "v170", "v171", "v172", "v173", "v174", "v175", "v176", "v177", "v178", "v179", "v180", "v181", "v182", "v183", "v184", "v185", "v186", "v187", "v188", "v189", "v190", "v191", "v192", "v193", "v194", "v195", "v196", "v197", "v198", "v199", "v200", "v201", "v202", "v203", "v204", "v205", "v206", "v207", "v208", "v209", "v210", "v211", "v212", "v213", "v214", "v215", "v216", "v217", "v218", "v219", "v220", "v221", "v222", "v223", "v224", "v225", "v226", "v227", "v228", "v229", "v230", "v231", "v232", "v233", "v234", "v235", "v236", "v237", "v238", "v239", "v240", "v241", "v242", "v243", "v244", "v245", "v246", "v247", "v248", "v249", "v250", "v251", "v252", "v253", "v254", "v255", "s4", "s5", "s6", "s7", "s8", "s9", "s10", "s11", "s12", "s13", "s14", "s15", "s16", "s17", "s18", "s19", "s20", "s21", "s22", "s23", "s24", "s25", "s26", "s27", "s28", "s29", "s30", "s31", "s33", "s34", "s35", "s36", "s37", "s38", "s39", "s40", "s41", "s42", "s43", "s44", "s45", "s46", "s47", "s48", "s49", "s50", "s51", "s52", "s53", "s54", "s55", "s56", "s57", "s58", "s59", "s60", "s61", "s62", "s63", "s64", "s65", "s66", "s67", "s68", "s69", "s70", "s71", "vcc", "scc", "memory");
}

extern "C" void kernel_launch(void* const* d_in, const int* in_sizes, int n_in,
                              void* d_out, int out_size, void* d_ws, size_t ws_size,
                              hipStream_t stream) {
    (void)hipFuncSetAttribute((const void*)sq_kernel, hipFuncAttributeMaxDynamicSharedMemorySize, 163840);
    sq_kernel<<<252, 512, 163840, stream>>>(d_in[0], d_in[1], d_in[2], d_out);
}
```
